```python
import torch

def _softmax(x, axis=-1):
    return torch.softmax(x, dim=axis)
def _gelu(x):
    return torch.nn.functional.gelu(x, approximate="tanh")
def _silu(x):
    return torch.nn.functional.silu(x)
def _sigmoid(x):
    return torch.sigmoid(x)
def _layernorm(x, axis=-1, eps=1e-5):
    m = x.mean(dim=axis, keepdim=True)
    v = x.var(dim=axis, keepdim=True, unbiased=False)
    return (x - m) / torch.sqrt(v + eps)
def _scatter_add(tgt, idx, src, axis=0):
    return tgt.clone().index_add_(axis, idx, src)
def _segment_sum(src, seg, n):
    out = torch.zeros((n,) + src.shape[1:], dtype=src.dtype)
    return out.index_add_(0, seg, src)
def _pad(x, pw):
    flat = tuple(v for pair in reversed(pw) for v in pair)
    return torch.nn.functional.pad(x, flat)

kernel_name = 'recurrent_scan__gqa_block_cached__mixer_token_mlp__decode'

def setup_inputs():

    _w = (lambda a: (lambda t: t.to(torch.bfloat16) if t.is_floating_point() else t)(torch.as_tensor(a)))
    def _A_setup():
        B, Sq, D = 4, 1, 576
        Hq, Hkv, Sk, Dh = 32, 16, 8192, 128
        assert Hq % Hkv == 0, (Hq, Hkv)
        return {
            "x": _w((lambda *s: torch.randn(*s, dtype=torch.bfloat16) * 0.05)(B, Sq, D)),
            "k_cache": _w((lambda *s: torch.randn(*s, dtype=torch.bfloat16) * 1.6)(B, Hkv, Sk, Dh)),
            "v_cache": _w((lambda *s: torch.randn(*s, dtype=torch.bfloat16) * 1.6)(B, Hkv, Sk, Dh)),
            "wq": _w((lambda *s: torch.randn(*s, dtype=torch.bfloat16) * 1.6)(D, Hq * Dh) * (D ** -0.5)),
            "wo": _w((lambda *s: torch.randn(*s, dtype=torch.bfloat16) * 1.6)(Hq * Dh, D) * ((Hq * Dh) ** -0.5)),
        }
    _A_in = _A_setup()

    def _B_setup():
        B, S, D, F = 4, 1, 576, 4608
        return {
            "x": _w((lambda *s: torch.randn(*s, dtype=torch.bfloat16) * 0.05)(B, S, D)),
            "w1": _w((lambda *s: torch.randn(*s, dtype=torch.bfloat16) * 0.2)(S, F) * (S ** -0.5)),
            "b1": _w((lambda *s: torch.randn(*s, dtype=torch.bfloat16) * 0.2)(F) * 0.02),
            "w2": _w((lambda *s: torch.randn(*s, dtype=torch.bfloat16) * 0.2)(F, S) * (F ** -0.5)),
            "b2": _w((lambda *s: torch.randn(*s, dtype=torch.bfloat16) * 0.2)(S) * 0.02),
        }
    _B_in = _B_setup()

    return {
        "A_x": _A_in["x"],
        "A_k_cache": _A_in["k_cache"],
        "A_v_cache": _A_in["v_cache"],
        "A_wq": _A_in["wq"],
        "A_wo": _A_in["wo"],
        "B_x": _B_in["x"],
        "B_w1": _B_in["w1"],
        "B_b1": _B_in["b1"],
        "B_w2": _B_in["w2"],
        "B_b2": _B_in["b2"],
    }

def reference(A_x, A_k_cache, A_v_cache, A_wq, A_wo, B_x, B_w1, B_b1, B_w2, B_b2):
    def _A_ref(x, k_cache, v_cache, wq, wo):
        B, Sq, D = x.shape
        Hq, Dh = 32, 128
        Hkv = k_cache.shape[1]
        rep = Hq // Hkv
        q = (lambda x, *d: x.permute(*d))((x @ wq).reshape(B, Sq, Hq, Dh), 0, 2, 1, 3)
        k = (lambda x, n, axis: torch.repeat_interleave(x, n, dim=axis))(k_cache, rep, axis=1)
        v = (lambda x, n, axis: torch.repeat_interleave(x, n, dim=axis))(v_cache, rep, axis=1)
        scores = torch.einsum("bhqd,bhkd->bhqk", q, k) * (Dh ** -0.5)
        p = _softmax(scores, axis=-1)
        o = torch.einsum("bhqk,bhkd->bhqd", p, v)
        return (lambda x, *d: x.permute(*d))(o, 0, 2, 1, 3).reshape(B, Sq, Hq * Dh) @ wo

    def _B_ref(x, w1, b1, w2, b2):
        B, S, D = x.shape
        h = _layernorm(x, axis=-1)
        u = torch.einsum("bsd,sf->bdf", h, w1) + b1
        u = _gelu(u)
        y = torch.einsum("bdf,fs->bds", u, w2) + b2
        return (lambda x, *d: x.permute(*d))(y, 0, 2, 1)

    x = A_x
    xn = _layernorm(x, axis=-1)
    s = _A_ref(xn, A_k_cache, A_v_cache, A_wq, A_wo)
    g = _silu(_B_ref(xn, B_w1, B_b1, B_w2, B_b2))
    return x + s * g
```

```cpp
#include <hip/hip_runtime.h>
#include <hip/hip_bf16.h>
#include <stdint.h>

namespace {

constexpr int NB = 4, DM = 576, HQ = 32, HKV = 16, SK = 8192, DH = 128, FF = 4608;
constexpr int NWG = 256, NTHR = 512, NWAVE = 8;
constexpr int NSPLIT = 4;
constexpr int KEYS_WG = SK / NSPLIT;
constexpr int PAGE = 32;
constexpr int NPG = KEYS_WG / PAGE / NWAVE;
constexpr int QN = HQ * DH;
constexpr float LOG2E = 1.4426950408889634f;
constexpr float SC2 = 0.08838834764831845f * LOG2E;
constexpr float EPS = 1e-5f;

constexpr int WS_EPOCH = 0;
constexpr int WS_FLAG = 4096;
constexpr int WS_REC = 65536;
constexpr int REC_BYTES = 1152;
constexpr int WS_BYTES = WS_REC + NWG * REC_BYTES;
constexpr unsigned TAG_STEP = 0x9E3779B1u;
constexpr unsigned SPIN_CAP = 1u << 20;

constexpr int L_XN = 0;
constexpr int L_QP = L_XN + NWAVE * DM;
constexpr int L_QB = L_QP + NWAVE * 256;
constexpr int L_H = L_QB + 128;
constexpr int L_MW = L_H + DM;
constexpr int L_MRG = L_MW + 3 * FF / 2;
constexpr int MRG_REC = 260;
constexpr int L_FIN = L_MRG + NWAVE * MRG_REC;
constexpr int L_TAG = L_FIN + 2 * NWAVE * 16;
constexpr int L_WORDS = L_TAG + 4;

typedef unsigned u32x4 __attribute__((ext_vector_type(4)));
typedef unsigned u32x2 __attribute__((ext_vector_type(2)));
typedef float f32x4 __attribute__((ext_vector_type(4)));
typedef __bf16 bf16x2 __attribute__((ext_vector_type(2)));
#define RLX_AGENT __ATOMIC_RELAXED, __HIP_MEMORY_SCOPE_AGENT
constexpr int AUX_SC1 = 16, AUX_NT = 2;

__device__ __forceinline__ float dot2(unsigned a, unsigned b, float acc) {
  return __builtin_amdgcn_fdot2_f32_bf16(__builtin_bit_cast(bf16x2, a), __builtin_bit_cast(bf16x2, b), acc, false);
}
__device__ __forceinline__ unsigned cvtpk(float lo, float hi) {
  bf16x2 r = {(__bf16)lo, (__bf16)hi};
  return __builtin_bit_cast(unsigned, r);
}
__device__ __forceinline__ float bf_lo(unsigned v) { return __builtin_bit_cast(float, v << 16); }
__device__ __forceinline__ float bf_hi(unsigned v) { return __builtin_bit_cast(float, v & 0xffff0000u); }
__device__ __forceinline__ float bf2f(unsigned short v) { return __builtin_bit_cast(float, (unsigned)v << 16); }
template <int CTRL> __device__ __forceinline__ float dpp(float x) {
  return __builtin_bit_cast(float, __builtin_amdgcn_mov_dpp(__builtin_bit_cast(int, x), CTRL, 0xf, 0xf, true));
}
constexpr int XOR1 = 0xB1, XOR2 = 0x4E, XOR7 = 0x141, ROR8 = 0x128, ROR4 = 0x124, BCAST = 0x150;
constexpr int QP0 = 0x00, QP1 = 0x55;
__device__ __forceinline__ float ex2(float x) { return __builtin_amdgcn_exp2f(x); }
__device__ __forceinline__ float rcp(float x) { return __builtin_amdgcn_rcpf(x); }

__device__ __forceinline__ float swap32_add(float a, float b) {
  auto s = __builtin_amdgcn_permlane32_swap(__float_as_uint(a), __float_as_uint(b), false, false);
  return __uint_as_float(s[0]) + __uint_as_float(s[1]);
}
__device__ __forceinline__ float swap16_add(float a, float b) {
  auto s = __builtin_amdgcn_permlane16_swap(__float_as_uint(a), __float_as_uint(b), false, false);
  return __uint_as_float(s[0]) + __uint_as_float(s[1]);
}
__device__ __forceinline__ float xrow_max(float x) {
  auto s = __builtin_amdgcn_permlane16_swap(__float_as_uint(x), __float_as_uint(x), false, false);
  x = fmaxf(__uint_as_float(s[0]), __uint_as_float(s[1]));
  auto t = __builtin_amdgcn_permlane32_swap(__float_as_uint(x), __float_as_uint(x), false, false);
  return fmaxf(__uint_as_float(t[0]), __uint_as_float(t[1]));
}
__device__ __forceinline__ float xrow_sum(float x) {
  x = swap16_add(x, x);
  return swap32_add(x, x);
}
__device__ __forceinline__ float wave_sum(float x) {
  x += dpp<XOR1>(x);
  x += dpp<XOR2>(x);
  x += dpp<XOR7>(x);
  x += dpp<ROR8>(x);
  return xrow_sum(x);
}
__device__ __forceinline__ float wave_treduce16(float (&v)[16], int lane) {
#pragma unroll
  for (int j = 0; j < 8; ++j) v[j] = swap32_add(v[j], v[j + 8]);
#pragma unroll
  for (int j = 0; j < 4; ++j) v[j] = swap16_add(v[j], v[j + 4]);
#pragma unroll
  for (int j = 0; j < 2; ++j) {
    const float a = v[j] + dpp<ROR8>(v[j]), b = v[j + 2] + dpp<ROR8>(v[j + 2]);
    v[j] = (lane & 8) ? b : a;
  }
  {
    const float a = v[0] + dpp<XOR7>(v[0]), b = v[1] + dpp<XOR7>(v[1]);
    v[0] = (lane & 4) ? b : a;
  }
  float r = v[0];
  r += dpp<XOR2>(r);
  r += dpp<XOR1>(r);
  return r;
}

#define SB() __builtin_amdgcn_sched_barrier(0)
#define PIN8(a) asm volatile("" : "+v"(a[0]), "+v"(a[1]), "+v"(a[2]), "+v"(a[3]), "+v"(a[4]), "+v"(a[5]), "+v"(a[6]), "+v"(a[7]) : : "memory")

__global__ void __launch_bounds__(NTHR, 2)
fused_decode_kernel(const unsigned short* __restrict__ x, const unsigned short* __restrict__ kc, const unsigned short* __restrict__ vc,
                    const unsigned short* __restrict__ wq, const unsigned short* __restrict__ wo, const unsigned short* __restrict__ w1,
                    const unsigned short* __restrict__ b1, const unsigned short* __restrict__ w2, const unsigned short* __restrict__ b2,
                    unsigned short* __restrict__ out, unsigned char* ws) {
  __shared__ __attribute__((aligned(16))) unsigned lds[L_WORDS];
  float* ldf = reinterpret_cast<float*>(lds);
  const int tid = threadIdx.x, lane = tid & 63, w = __builtin_amdgcn_readfirstlane(tid >> 6);
  const int r = lane >> 4, c = lane & 15;
  const int bid = blockIdx.x, xcd = bid & 7, ii = bid >> 3;
  const int kvh = xcd * 2 + (ii >> 4), b = (ii >> 2) & 3, sp = ii & 3;
  const int prod = b * 64 + kvh * 4 + sp;
  const int u = ((ii >> 4) << 2) | sp;
  int cg = -1;
  if (u < 4) cg = xcd * 4 + u;
  else if (u < 6 && (xcd >> 1) == b) cg = 32 + ((xcd & 1) << 1) + (u - 4);
  const bool fin = cg >= 0;
  const int col0 = cg * 16;

  const unsigned epoch = __hip_atomic_load(reinterpret_cast<unsigned*>(ws + WS_EPOCH), RLX_AGENT);
  unsigned short xr[9];
#pragma unroll
  for (int j = 0; j < 9; ++j) xr[j] = x[b * DM + j * 64 + lane];
  const size_t kvbase = ((size_t)(b * HKV + kvh) * SK + (size_t)sp * KEYS_WG) * DH;
  const __amdgpu_buffer_rsrc_t rk = __builtin_amdgcn_make_buffer_rsrc((void*)(kc + kvbase), 0, KEYS_WG * DH * 2, 0x00020000);
  const __amdgpu_buffer_rsrc_t rv = __builtin_amdgcn_make_buffer_rsrc((void*)(vc + kvbase), 0, KEYS_WG * DH * 2, 0x00020000);
  const __amdgpu_buffer_rsrc_t rwq = __builtin_amdgcn_make_buffer_rsrc((void*)wq, 0, DM * QN * 2, 0x00020000);
  const __amdgpu_buffer_rsrc_t rwo = __builtin_amdgcn_make_buffer_rsrc((void*)wo, 0, DM * QN * 2, 0x00020000);
  constexpr int PBYTES = PAGE * DH * 2;
  constexpr int PSTR = NWAVE * PBYTES;
  const int lo16 = lane * 16;
  const int wpo = w * PBYTES;
#define LDK(i, t) __builtin_amdgcn_raw_buffer_load_b128(rk, lo16 + ((t) & 3) * 1024, wpo + (i) * PSTR + ((t) >> 2) * 4096, AUX_NT)
#define LDV(i, t) __builtin_amdgcn_raw_buffer_load_b128(rv, lo16 + ((t) & 3) * 1024, wpo + (i) * PSTR + ((t) >> 2) * 4096, AUX_NT)
  u32x4 kb[8], vb[8];
  u32x4 wqr[36];
  const int wqv = (36 * (lane >> 5)) * QN * 2 + (lane & 31) * 16;
  const int wqs = (72 * w * QN + kvh * 256) * 2;
  SB();
#pragma unroll
  for (int t = 0; t < 8; ++t) {
    kb[t] = LDK(0, t);
#pragma unroll
    for (int j = 0; j < 4; ++j) wqr[t * 4 + j] = __builtin_amdgcn_raw_buffer_load_b128(rwq, wqv, wqs + (t * 4 + j) * QN * 2, 0);
    SB();
  }
#pragma unroll
  for (int j = 32; j < 36; ++j) wqr[j] = __builtin_amdgcn_raw_buffer_load_b128(rwq, wqv, wqs + j * QN * 2, 0);
  SB();
#pragma unroll
  for (int t = 0; t < 8; ++t) vb[t] = LDV(0, t);
  SB();

  {
    float xv[9];
    float s = 0.f;
#pragma unroll
    for (int j = 0; j < 9; ++j) { xv[j] = bf2f(xr[j]); s += xv[j]; }
    const float mean = wave_sum(s) * (1.0f / DM);
    float v = 0.f;
#pragma unroll
    for (int j = 0; j < 9; ++j) { xv[j] -= mean; v = fmaf(xv[j], xv[j], v); }
    const float rstd = 1.0f / sqrtf(wave_sum(v) * (1.0f / DM) + EPS);
    float s2 = 0.f;
#pragma unroll
    for (int j = 0; j < 9; ++j) {
      const unsigned xb = cvtpk(xv[j] * rstd, 0.f) & 0xffffu;
      lds[L_XN + w * DM + j * 64 + lane] = xb;
      xv[j] = bf_lo(xb);
      s2 += xv[j];
    }
    if (w == 0) {
      const float mean2 = wave_sum(s2) * (1.0f / DM);
      float v2 = 0.f;
#pragma unroll
      for (int j = 0; j < 9; ++j) { xv[j] -= mean2; v2 = fmaf(xv[j], xv[j], v2); }
      const float rstd2 = 1.0f / sqrtf(wave_sum(v2) * (1.0f / DM) + EPS);
#pragma unroll
      for (int j = 0; j < 9; ++j) ldf[L_H + j * 64 + lane] = xv[j] * rstd2;
      if (tid == 0) lds[L_TAG] = epoch + TAG_STEP;
    }
  }
  {
    float qa[8];
#pragma unroll
    for (int k = 0; k < 8; ++k) qa[k] = 0.f;
    const u32x4* xs = reinterpret_cast<const u32x4*>(lds + L_XN + w * DM + 72 * w + 36 * (lane >> 5));
#pragma unroll
    for (int j4 = 0; j4 < 9; ++j4) {
      const u32x4 xq = xs[j4];
#pragma unroll
      for (int jj = 0; jj < 4; ++jj) {
        const unsigned xl = xq[jj], xh = xq[jj] << 16;
        const u32x4 wv = wqr[j4 * 4 + jj];
#pragma unroll
        for (int k = 0; k < 4; ++k) {
          qa[2 * k] = dot2(wv[k], xl, qa[2 * k]);
          qa[2 * k + 1] = dot2(wv[k], xh, qa[2 * k + 1]);
        }
      }
    }
    f32x4 qs;
#pragma unroll
    for (int k = 0; k < 4; ++k) qs[k] = swap32_add(qa[k], qa[k + 4]);
    *reinterpret_cast<f32x4*>(ldf + L_QP + w * 256 + (lane & 31) * 8 + (lane >> 5) * 4) = qs;
  }
  SB();
  u32x4 wor[16];
  u32x4 mw[4];
  if (fin) {
    const int wov = (8 * tid * DM) * 2;
#pragma unroll
    for (int j = 0; j < 8; ++j) {
      wor[2 * j] = __builtin_amdgcn_raw_buffer_load_b128(rwo, wov, col0 * 2 + j * DM * 2, 0);
      wor[2 * j + 1] = __builtin_amdgcn_raw_buffer_load_b128(rwo, wov + 16, col0 * 2 + j * DM * 2, 0);
    }
#pragma unroll
    for (int j = 0; j < 3; ++j) {
      const unsigned short* src = j == 0 ? w1 : (j == 1 ? b1 : w2);
      mw[j] = reinterpret_cast<const u32x4*>(src)[72 * w + lane];
    }
    {
      const int a = lane >> 3;
      const unsigned short* src = a == 0 ? w1 : (a == 1 ? b1 : w2);
      if (lane < 24) mw[3] = reinterpret_cast<const u32x4*>(src)[72 * w + 64 + (lane & 7)];
    }
  } else {
#pragma unroll
    for (int j = 0; j < 16; ++j) wor[j] = u32x4{0u, 0u, 0u, 0u};
  }
  SB();
  __syncthreads();
  if (tid < 128) {
    float q0 = 0.f, q1 = 0.f;
#pragma unroll
    for (int ww = 0; ww < NWAVE; ++ww) {
      const float2 t = *reinterpret_cast<const float2*>(ldf + L_QP + ww * 256 + tid * 2);
      q0 += t.x; q1 += t.y;
    }
    lds[L_QB + tid] = cvtpk(q0, q1);
  }
  __syncthreads();
  const u32x4 qh0 = *reinterpret_cast<const u32x4*>(lds + L_QB + c * 4);
  const u32x4 qh1 = *reinterpret_cast<const u32x4*>(lds + L_QB + 64 + c * 4);
  const unsigned tag = lds[L_TAG];
  float hv[16];
  float ya[16];
#pragma unroll
  for (int j = 0; j < 16; ++j) {
    ya[j] = 0.f;
    hv[j] = __builtin_bit_cast(float, __builtin_amdgcn_readfirstlane(lds[L_H + (fin ? col0 : 0) + j]));
  }
  const unsigned short* mwl = reinterpret_cast<const unsigned short*>(lds + L_MW) + w * (3 * 576);

  auto mlp_slice = [&](int k) {
    const int f = k * 64 + lane;
    const float w1f = bf2f(mwl[f]), b1f = bf2f(mwl[576 + f]), w2f = bf2f(mwl[2 * 576 + f]);
    constexpr float C1 = -2.0f * 0.7978845608028654f * LOG2E, C2 = C1 * 0.044715f;
#pragma unroll
    for (int j = 0; j < 16; ++j) {
      const float uu = fmaf(hv[j], w1f, b1f);
      const float e = ex2(uu * fmaf(uu * uu, C2, C1));
      ya[j] = fmaf(uu * w2f, rcp(1.0f + e), ya[j]);
    }
  };

  float m = -INFINITY, l = 0.f;
  float a0[8], a1[8];
#pragma unroll
  for (int k = 0; k < 8; ++k) { a0[k] = 0.f; a1[k] = 0.f; }

  auto page = [&](int i, bool next) {
    float xs[16];
#pragma unroll
    for (int t = 0; t < 8; ++t) {
      float d0 = 0.f, d1 = 0.f;
#pragma unroll
      for (int k = 0; k < 4; ++k) { d0 = dot2(kb[t][k], qh0[k], d0); d1 = dot2(kb[t][k], qh1[k], d1); }
      xs[2 * t] = d0; xs[2 * t + 1] = d1;
    }
    PIN8(xs); PIN8((xs + 8));
    SB();
    if (next) {
#pragma unroll
      for (int t = 0; t < 8; ++t) kb[t] = LDK(i + 1, t);
    }
    SB();
#define TR_STEP(HALF, CTRL, BIT) _Pragma("unroll") for (int h = 0; h < (HALF); ++h) { \
      const float t1_ = xs[h] + dpp<CTRL>(xs[h]), t2_ = xs[h + (HALF)] + dpp<CTRL>(xs[h + (HALF)]); \
      xs[h] = (c & (BIT)) ? t2_ : t1_; }
    TR_STEP(8, ROR8, 8) TR_STEP(4, XOR7, 4) TR_STEP(2, XOR2, 2) TR_STEP(1, XOR1, 1)
#undef TR_STEP
    const float sc = xs[0] * SC2;
    float pm = fmaxf(sc, dpp<XOR2>(sc));
    pm = fmaxf(pm, dpp<ROR4>(pm));
    pm = fmaxf(pm, dpp<ROR8>(pm));
    const float mn = fmaxf(m, pm);
    const float alpha = ex2(m - mn), p = ex2(sc - mn);
    l = fmaf(l, alpha, p);
    m = mn;
    const float al0 = dpp<QP0>(alpha), al1 = dpp<QP1>(alpha);
#pragma unroll
    for (int k = 0; k < 8; ++k) { a0[k] *= al0; a1[k] *= al1; }
#define PV_STEP(t) { const float p0 = dpp<BCAST + 2 * (t)>(p), p1 = dpp<BCAST + 2 * (t) + 1>(p); \
      _Pragma("unroll") for (int k = 0; k < 4; ++k) { const float lo = bf_lo(vb[t][k]), hi = bf_hi(vb[t][k]); \
        a0[2 * k] = fmaf(p0, lo, a0[2 * k]); a0[2 * k + 1] = fmaf(p0, hi, a0[2 * k + 1]); \
        a1[2 * k] = fmaf(p1, lo, a1[2 * k]); a1[2 * k + 1] = fmaf(p1, hi, a1[2 * k + 1]); } }
    PV_STEP(0) PV_STEP(1) PV_STEP(2) PV_STEP(3) PV_STEP(4) PV_STEP(5) PV_STEP(6) PV_STEP(7)
#undef PV_STEP
    PIN8(a0); PIN8(a1);
    SB();
    if (next) {
#pragma unroll
      for (int t = 0; t < 8; ++t) vb[t] = LDV(i + 1, t);
    }
    SB();
  };

  page(0, true);
  if (fin) {
    unsigned* mwd = lds + L_MW + w * (3 * 288);
#pragma unroll
    for (int j = 0; j < 3; ++j) *reinterpret_cast<u32x4*>(mwd + j * 288 + lane * 4) = mw[j];
    if (lane < 24) *reinterpret_cast<u32x4*>(mwd + (lane >> 3) * 288 + 256 + (lane & 7) * 4) = mw[3];
    mlp_slice(0);
  }
  SB();
#pragma unroll 1
  for (int i = 1; i < NPG - 1; ++i) {
    page(i, true);
    if (fin) mlp_slice(i);
    SB();
  }
  if (fin) {
    mlp_slice(7);
    mlp_slice(8);
    const float ysum = wave_treduce16(ya, lane);
    if ((lane & 3) == 0) ldf[L_FIN + 128 + w * 16 + (lane >> 2)] = ysum;
#pragma unroll
    for (int rp = 0; rp < 4; ++rp) {
#pragma unroll
      for (int hf = 0; hf < 2; ++hf) {
        const u32x4 ra = wor[4 * rp + hf], rb = wor[4 * rp + 2 + hf];
        u32x4 lo, hi;
#pragma unroll
        for (int k = 0; k < 4; ++k) {
          lo[k] = __builtin_amdgcn_perm(rb[k], ra[k], 0x05040100u);
          hi[k] = __builtin_amdgcn_perm(rb[k], ra[k], 0x07060302u);
        }
        wor[4 * rp + hf] = lo;
        wor[4 * rp + 2 + hf] = hi;
      }
    }
  }
  SB();
  page(NPG - 1, false);

  {
    const float mw_ = xrow_max(m);
    const float sc_ = ex2(m - mw_);
    float lr = l * sc_;
    lr += dpp<XOR2>(lr);
    lr += dpp<ROR4>(lr);
    lr += dpp<ROR8>(lr);
    lr = xrow_sum(lr);
    const float s0 = dpp<QP0>(sc_), s1 = dpp<QP1>(sc_);
    float t8[8];
#pragma unroll
    for (int k = 0; k < 8; ++k) t8[k] = swap32_add(a0[k] * s0, a1[k] * s1);
    f32x4 t4;
#pragma unroll
    for (int k = 0; k < 4; ++k) t4[k] = swap16_add(t8[k], t8[k + 4]);
    float* rec = ldf + L_MRG + w * MRG_REC;
    *reinterpret_cast<f32x4*>(rec + (lane >> 5) * 128 + c * 8 + (r & 1) * 4) = t4;
    if (lane < 2) { rec[256 + lane * 2] = mw_; rec[257 + lane * 2] = lr; }
  }
  __syncthreads();
  const __amdgpu_buffer_rsrc_t rs = __builtin_amdgcn_make_buffer_rsrc((void*)ws, 0, WS_BYTES, 0x00020000);
  if (w == 0) {
    const int hh = lane >> 5;
    float mm[NWAVE], ll[NWAVE];
    float M = -INFINITY;
#pragma unroll
    for (int ww = 0; ww < NWAVE; ++ww) {
      const float2 t = *reinterpret_cast<const float2*>(ldf + L_MRG + ww * MRG_REC + 256 + hh * 2);
      mm[ww] = t.x; ll[ww] = t.y; M = fmaxf(M, t.x);
    }
    f32x4 O = {0.f, 0.f, 0.f, 0.f};
    float L = 0.f;
#pragma unroll
    for (int ww = 0; ww < NWAVE; ++ww) {
      const float wt = ex2(mm[ww] - M);
      L = fmaf(wt, ll[ww], L);
      O += wt * *reinterpret_cast<const f32x4*>(ldf + L_MRG + ww * MRG_REC + lane * 4);
    }
    const int ro = WS_REC + prod * REC_BYTES;
    __builtin_amdgcn_raw_buffer_store_b128(__builtin_bit_cast(u32x4, O), rs, ro + lane * 16, 0, AUX_SC1);
    if ((lane & 31) == 0) {
      u32x2 ml = {__float_as_uint(M), __float_as_uint(L)};
      __builtin_amdgcn_raw_buffer_store_b64(ml, rs, ro + 1024 + hh * 8, 0, AUX_SC1);
    }
    asm volatile("s_waitcnt vmcnt(0)" ::: "memory");
    if (lane == 0) __hip_atomic_store(reinterpret_cast<unsigned*>(ws + WS_FLAG + prod * 128), tag, RLX_AGENT);
  }

  if (fin) {
    {
      const unsigned* fp = reinterpret_cast<const unsigned*>(ws + WS_FLAG + (b * 64 + 8 * w + (lane & 7)) * 128);
      for (unsigned spins = 0; spins < SPIN_CAP; ++spins) {
        const unsigned v = __hip_atomic_load(fp, RLX_AGENT);
        if (__all(v == tag)) break;
        __builtin_amdgcn_s_sleep(1);
      }
      __builtin_amdgcn_fence(__ATOMIC_ACQUIRE, "wavefront");
    }
    const int p0 = b * 64 + 8 * w + (r >> 1) * 4;
    u32x4 oa[4], ob[4];
    u32x2 ml[4];
#pragma unroll
    for (int s = 0; s < 4; ++s) {
      const int ro = WS_REC + (p0 + s) * REC_BYTES;
      oa[s] = __builtin_amdgcn_raw_buffer_load_b128(rs, ro + (r & 1) * 512 + c * 32, 0, AUX_SC1);
      ob[s] = __builtin_amdgcn_raw_buffer_load_b128(rs, ro + (r & 1) * 512 + c * 32 + 16, 0, AUX_SC1);
      ml[s] = __builtin_amdgcn_raw_buffer_load_b64(rs, ro + 1024 + (r & 1) * 8, 0, AUX_SC1);
    }
    float M = -INFINITY;
#pragma unroll
    for (int s = 0; s < 4; ++s) M = fmaxf(M, __uint_as_float(ml[s][0]));
    float L = 0.f;
    f32x4 o0 = {0.f, 0.f, 0.f, 0.f}, o1 = {0.f, 0.f, 0.f, 0.f};
#pragma unroll
    for (int s = 0; s < 4; ++s) {
      const float wt = ex2(__uint_as_float(ml[s][0]) - M);
      L = fmaf(wt, __uint_as_float(ml[s][1]), L);
      o0 += wt * __builtin_bit_cast(f32x4, oa[s]);
      o1 += wt * __builtin_bit_cast(f32x4, ob[s]);
    }
    const float inv = 1.0f / L;
    unsigned op[4];
    op[0] = cvtpk(o0[0] * inv, o0[1] * inv); op[1] = cvtpk(o0[2] * inv, o0[3] * inv);
    op[2] = cvtpk(o1[0] * inv, o1[1] * inv); op[3] = cvtpk(o1[2] * inv, o1[3] * inv);
    float sv[16];
#pragma unroll
    for (int hf = 0; hf < 2; ++hf)
#pragma unroll
      for (int k = 0; k < 4; ++k) {
        float e = 0.f, o = 0.f;
#pragma unroll
        for (int rp = 0; rp < 4; ++rp) {
          e = dot2(wor[4 * rp + hf][k], op[rp], e);
          o = dot2(wor[4 * rp + 2 + hf][k], op[rp], o);
        }
        sv[8 * hf + 2 * k] = e;
        sv[8 * hf + 2 * k + 1] = o;
      }
    const float ssum = wave_treduce16(sv, lane);
    if ((lane & 3) == 0) ldf[L_FIN + w * 16 + (lane >> 2)] = ssum;
    __syncthreads();
    if (tid < 16) {
      float s = 0.f, y = 0.f;
#pragma unroll
      for (int ww = 0; ww < NWAVE; ++ww) { s += ldf[L_FIN + ww * 16 + tid]; y += ldf[L_FIN + 128 + ww * 16 + tid]; }
      y += bf2f(b2[0]);
      const float g = y / (1.0f + __expf(-y));
      const float xo = bf2f(x[b * DM + col0 + tid]);
      out[b * DM + col0 + tid] = (unsigned short)(cvtpk(fmaf(s, g, xo), 0.f) & 0xffffu);
    }
  } else if (bid == NWG - 1 && w == 1) {
    bool ok = false;
    for (unsigned spins = 0; spins < SPIN_CAP && !ok; ++spins) {
      bool a = true;
#pragma unroll
      for (int j = 0; j < 4; ++j)
        a &= __hip_atomic_load(reinterpret_cast<const unsigned*>(ws + WS_FLAG + (j * 64 + lane) * 128), RLX_AGENT) == tag;
      ok = __all(a);
      if (!ok) __builtin_amdgcn_s_sleep(2);
    }
    if (lane == 0) __hip_atomic_store(reinterpret_cast<unsigned*>(ws + WS_EPOCH), tag, RLX_AGENT);
  }
}

}

extern "C" void kernel_launch(void* const* d_in, const int* in_sizes, int n_in, void* d_out, int out_size, void* d_ws, size_t ws_size,
                              hipStream_t stream) {
  (void)in_sizes; (void)n_in; (void)out_size; (void)ws_size;
  fused_decode_kernel<<<NWG, NTHR, 0, stream>>>(
      (const unsigned short*)d_in[0], (const unsigned short*)d_in[1], (const unsigned short*)d_in[2], (const unsigned short*)d_in[3],
      (const unsigned short*)d_in[4], (const unsigned short*)d_in[6], (const unsigned short*)d_in[7], (const unsigned short*)d_in[8],
      (const unsigned short*)d_in[9], (unsigned short*)d_out, (unsigned char*)d_ws);
}
```

```cpp
#include <hip/hip_runtime.h>
#include <hip/hip_bf16.h>
#include <stdint.h>

#define FAIR 0
#define DEPTH 2
#define BO_T1 16
#define BO_T2 8
#define BO_S 16
#define MAPV 3
#define ROTV 0

namespace {

constexpr int NB = 4, DM = 576, HQ = 32, HKV = 16, SK = 8192, DH = 128, FF = 4608;
constexpr int NWG = 256, NTHR = 512, NWAVE = 8;
constexpr int NSPLIT = 4;
constexpr int KEYS_WG = SK / NSPLIT;
constexpr int PAGE = 32;
constexpr int NPG = KEYS_WG / PAGE / NWAVE;
constexpr int QN = HQ * DH;
constexpr float LOG2E = 1.4426950408889634f;
constexpr float SC2 = 0.08838834764831845f * LOG2E;
constexpr float EPS = 1e-5f;
constexpr int CB = 72;
constexpr int NOUT = 9;

constexpr int WS_EPOCH = 0;
constexpr int WS_QG = 4096;
constexpr int WS_OG = WS_QG + NB * 4 * QN * 8;
constexpr int OREC = 1152;
constexpr int WS_SG = WS_OG + NWG * 2 * OREC;
constexpr int WS_BYTES = WS_SG + NB * DM * HQ * 8;
constexpr unsigned TAG_STEP = 0x9E3779B1u;
constexpr unsigned SPIN_CAP = 1u << 20;

constexpr int L_XN = 0;
constexpr int L_QP = L_XN + DM * 4;
constexpr int L_QB = L_QP + NWAVE * 256;
constexpr int L_FLAG = L_QB + 128;
constexpr int L_H = L_FLAG + 4;
constexpr int L_X = L_H + DM;
constexpr int L_MW = L_X + DM;
constexpr int MW_W = 3 * 288;
constexpr int L_WT = L_MW + NWAVE * MW_W;
constexpr int L_O = L_WT + DH * CB / 2 + 8;
constexpr int L_Y = L_O + NB * DH / 2;
constexpr int L_WORDS = L_Y + NWAVE * 16;
static_assert(L_WORDS * 4 <= 163840, "LDS");
static_assert((L_QP % 4 | L_QB % 4 | L_MW % 4 | L_WT % 4 | L_O % 4 | L_FLAG % 2) == 0, "align");

typedef unsigned u32x4 __attribute__((ext_vector_type(4)));
typedef unsigned u32x2 __attribute__((ext_vector_type(2)));
typedef float f32x4 __attribute__((ext_vector_type(4)));
typedef __bf16 bf16x2 __attribute__((ext_vector_type(2)));
typedef __bf16 bf16x8 __attribute__((ext_vector_type(8)));
#define RLX_AGENT __ATOMIC_RELAXED, __HIP_MEMORY_SCOPE_AGENT
constexpr int AUX_SC1 = 16, AUX_NT = 2;

__device__ __forceinline__ float dot2(unsigned a, unsigned b, float acc) {
  return __builtin_amdgcn_fdot2_f32_bf16(__builtin_bit_cast(bf16x2, a), __builtin_bit_cast(bf16x2, b), acc, false);
}
__device__ __forceinline__ unsigned cvtpk(float lo, float hi) {
  bf16x2 r = {(__bf16)lo, (__bf16)hi};
  return __builtin_bit_cast(unsigned, r);
}
__device__ __forceinline__ float bf_lo(unsigned v) { return __builtin_bit_cast(float, v << 16); }
__device__ __forceinline__ float bf_hi(unsigned v) { return __builtin_bit_cast(float, v & 0xffff0000u); }
__device__ __forceinline__ float bf2f(unsigned short v) { return __builtin_bit_cast(float, (unsigned)v << 16); }
template <int CTRL> __device__ __forceinline__ float dpp(float x) {
  return __builtin_bit_cast(float, __builtin_amdgcn_mov_dpp(__builtin_bit_cast(int, x), CTRL, 0xf, 0xf, true));
}
constexpr int XOR1 = 0xB1, XOR2 = 0x4E, XOR7 = 0x141, ROR8 = 0x128, ROR4 = 0x124, BCAST = 0x150;
constexpr int QP0 = 0x00, QP1 = 0x55;
__device__ __forceinline__ float ex2(float x) { return __builtin_amdgcn_exp2f(x); }
__device__ __forceinline__ float rcp(float x) { return __builtin_amdgcn_rcpf(x); }

__device__ __forceinline__ float swap32_add(float a, float b) {
  auto s = __builtin_amdgcn_permlane32_swap(__float_as_uint(a), __float_as_uint(b), false, false);
  return __uint_as_float(s[0]) + __uint_as_float(s[1]);
}
__device__ __forceinline__ float swap16_add(float a, float b) {
  auto s = __builtin_amdgcn_permlane16_swap(__float_as_uint(a), __float_as_uint(b), false, false);
  return __uint_as_float(s[0]) + __uint_as_float(s[1]);
}
__device__ __forceinline__ float xrow_max(float x) {
  auto s = __builtin_amdgcn_permlane16_swap(__float_as_uint(x), __float_as_uint(x), false, false);
  x = fmaxf(__uint_as_float(s[0]), __uint_as_float(s[1]));
  auto t = __builtin_amdgcn_permlane32_swap(__float_as_uint(x), __float_as_uint(x), false, false);
  return fmaxf(__uint_as_float(t[0]), __uint_as_float(t[1]));
}
__device__ __forceinline__ float xrow_sum(float x) {
  x = swap16_add(x, x);
  return swap32_add(x, x);
}
__device__ __forceinline__ float row_sum(float x) {
  x += dpp<XOR1>(x);
  x += dpp<XOR2>(x);
  x += dpp<XOR7>(x);
  x += dpp<ROR8>(x);
  return x;
}
__device__ __forceinline__ float wave_sum(float x) { return xrow_sum(row_sum(x)); }
__device__ __forceinline__ float wave_treduce16(float (&v)[16], int lane) {
#pragma unroll
  for (int j = 0; j < 8; ++j) v[j] = swap32_add(v[j], v[j + 8]);
#pragma unroll
  for (int j = 0; j < 4; ++j) v[j] = swap16_add(v[j], v[j + 4]);
#pragma unroll
  for (int j = 0; j < 2; ++j) {
    const float a = v[j] + dpp<ROR8>(v[j]), b = v[j + 2] + dpp<ROR8>(v[j + 2]);
    v[j] = (lane & 8) ? b : a;
  }
  {
    const float a = v[0] + dpp<XOR7>(v[0]), b = v[1] + dpp<XOR7>(v[1]);
    v[0] = (lane & 4) ? b : a;
  }
  float r = v[0];
  r += dpp<XOR2>(r);
  r += dpp<XOR1>(r);
  return r;
}

#define SB() __builtin_amdgcn_sched_barrier(0)
#define STAMP(n)
#define PIN8(a) asm volatile("" : "+v"(a[0]), "+v"(a[1]), "+v"(a[2]), "+v"(a[3]), "+v"(a[4]), "+v"(a[5]), "+v"(a[6]), "+v"(a[7]) : : "memory")
#define LDS_BARRIER() do { asm volatile("s_waitcnt lgkmcnt(0)" ::: "memory"); __builtin_amdgcn_s_barrier(); asm volatile("" ::: "memory"); } while (0)

__global__ void __launch_bounds__(NTHR, 2)
fused_decode_kernel(const unsigned short* __restrict__ x, const unsigned short* __restrict__ kc, const unsigned short* __restrict__ vc,
                    const unsigned short* __restrict__ wq, const unsigned short* __restrict__ wo, const unsigned short* __restrict__ w1,
                    const unsigned short* __restrict__ b1, const unsigned short* __restrict__ w2, const unsigned short* __restrict__ b2,
                    unsigned short* __restrict__ out, unsigned char* ws) {
  __shared__ __attribute__((aligned(16))) unsigned lds[L_WORDS];
  float* ldf = reinterpret_cast<float*>(lds);
  const int tid = threadIdx.x, lane = tid & 63, w = __builtin_amdgcn_readfirstlane(tid >> 6);
  const int r = lane >> 4, c = lane & 15;
  const int bid = blockIdx.x, xcd = bid & 7, ii = bid >> 3;
  const int kvh = ii & 15, b = (xcd >> 1) & 3, sp = ((xcd & 1) << 1) | (ii >> 4);
  const int rot = 0;
  const int prod = b * 64 + kvh * 4 + sp;
  const int col0 = (prod & 63) * NOUT;
  const int h1 = 4 * xcd + (ii >> 3), cbk = ii & 7;

  STAMP(0);
  const unsigned tag = *reinterpret_cast<const __attribute__((address_space(4))) unsigned*>(reinterpret_cast<uintptr_t>(ws + WS_EPOCH)) + TAG_STEP;
  const int lnb = w < 4 ? w : b;
  const __amdgpu_buffer_rsrc_t rx = __builtin_amdgcn_make_buffer_rsrc((void*)x, 0, NB * DM * 2, 0x00020000);
  const __amdgpu_buffer_rsrc_t rb2 = __builtin_amdgcn_make_buffer_rsrc((void*)b2, 0, 2, 0x00020000);
  unsigned short xr[9];
#pragma unroll
  for (int j = 0; j < 9; ++j) xr[j] = __builtin_amdgcn_raw_buffer_load_b16(rx, lane * 2 + j * 128, w <= 4 ? lnb * DM * 2 : 0x10000, 0);
  const unsigned short b2r = __builtin_amdgcn_raw_buffer_load_b16(rb2, 0, w == 4 ? 0 : 0x10000, 0);
  const size_t kvbase = ((size_t)(b * HKV + kvh) * SK + (size_t)sp * KEYS_WG) * DH;
  const __amdgpu_buffer_rsrc_t rk = __builtin_amdgcn_make_buffer_rsrc((void*)(kc + kvbase), 0, KEYS_WG * DH * 2, 0x00020000);
  const __amdgpu_buffer_rsrc_t rv = __builtin_amdgcn_make_buffer_rsrc((void*)(vc + kvbase), 0, KEYS_WG * DH * 2, 0x00020000);
  const __amdgpu_buffer_rsrc_t rwq = __builtin_amdgcn_make_buffer_rsrc((void*)wq, 0, DM * QN * 2, 0x00020000);
  const __amdgpu_buffer_rsrc_t rwo = __builtin_amdgcn_make_buffer_rsrc((void*)wo, 0, DM * QN * 2, 0x00020000);
  const __amdgpu_buffer_rsrc_t rs = __builtin_amdgcn_make_buffer_rsrc((void*)ws, 0, WS_BYTES, 0x00020000);
  constexpr int PBYTES = PAGE * DH * 2;
  constexpr int PSTR = NWAVE * PBYTES;
  const int lo16 = lane * 16;
  const int wpo = w * PBYTES + rot * PSTR;
#define LDK(i, t) __builtin_amdgcn_raw_buffer_load_b128(rk, lo16 + ((t) & 3) * 1024, wpo + (i) * PSTR + ((t) >> 2) * 4096, AUX_NT)
#define LDV(i, t) __builtin_amdgcn_raw_buffer_load_b128(rv, lo16 + ((t) & 3) * 1024, wpo + (i) * PSTR + ((t) >> 2) * 4096, AUX_NT)
  u32x4 kb0[8], vb0[8], kb1[8], vb1[8];
  const int cb = bid & 63, ks = bid >> 6;
  const int qrow = ks * 144 + w * 18 + (lane >> 3);
  u32x4 wqr[3];
  {
    const int wqv = (lane >> 3) * QN * 2 + (lane & 7) * 16;
    const int wqs = ((ks * 144 + w * 18) * QN + cb * 64) * 2;
    wqr[0] = __builtin_amdgcn_raw_buffer_load_b128(rwq, wqv, wqs, AUX_NT);
    wqr[1] = __builtin_amdgcn_raw_buffer_load_b128(rwq, wqv, wqs + 8 * QN * 2, AUX_NT);
    wqr[2] = __builtin_amdgcn_raw_buffer_load_b128(rwq, lane < 16 ? wqv : 0x7fffffff, wqs + 16 * QN * 2, AUX_NT);
  }
  SB();
  asm volatile("" ::: "memory");
  __builtin_amdgcn_s_barrier();
  asm volatile("" ::: "memory");
  SB();
#pragma unroll
  for (int t = 0; t < 8; ++t) { kb0[t] = LDK(0, t); SB(); }
  asm volatile("" :: "v"(xr[0]), "v"(xr[1]), "v"(xr[2]), "v"(xr[3]), "v"(xr[4]), "v"(xr[5]), "v"(xr[6]), "v"(xr[7]), "v"(xr[8]), "v"(b2r));
  SB();

  if (w <= 4) {
    float xv[9];
    float s = 0.f;
#pragma unroll
    for (int j = 0; j < 9; ++j) { xv[j] = bf2f(xr[j]); s += xv[j]; }
    if (w == 4) {
#pragma unroll
      for (int j = 0; j < 9; ++j) ldf[L_X + j * 64 + lane] = xv[j];
      ldf[L_FLAG + 2] = bf2f(b2r);
    }
    const float mean = wave_sum(s) * (1.0f / DM);
    float v = 0.f;
#pragma unroll
    for (int j = 0; j < 9; ++j) { xv[j] -= mean; v = fmaf(xv[j], xv[j], v); }
    const float rstd = 1.0f / sqrtf(wave_sum(v) * (1.0f / DM) + EPS);
    if (w < 4) {
#pragma unroll
      for (int j = 0; j < 9; ++j) lds[L_XN + (j * 64 + lane) * 4 + w] = cvtpk(xv[j] * rstd, 0.f) & 0xffffu;
    } else {
      float s2 = 0.f;
#pragma unroll
      for (int j = 0; j < 9; ++j) { xv[j] = bf_lo(cvtpk(xv[j] * rstd, 0.f)); s2 += xv[j]; }
      const float mean2 = wave_sum(s2) * (1.0f / DM);
      float v2 = 0.f;
#pragma unroll
      for (int j = 0; j < 9; ++j) { xv[j] -= mean2; v2 = fmaf(xv[j], xv[j], v2); }
      const float rstd2 = 1.0f / sqrtf(wave_sum(v2) * (1.0f / DM) + EPS);
#pragma unroll
      for (int j = 0; j < 9; ++j) ldf[L_H + j * 64 + lane] = xv[j] * rstd2;
    }
  } else if (w == 5) {
    if (lane < 2) lds[L_FLAG + lane] = 0u;
  }
  LDS_BARRIER();
  STAMP(1);
  {
    float qa[32];
#pragma unroll
    for (int k = 0; k < 32; ++k) qa[k] = 0.f;
#pragma unroll
    for (int j = 0; j < 3; ++j) {
      const int row = (j < 2) ? qrow + 8 * j : min(qrow + 16, DM - 1);
      const u32x4 xq = *reinterpret_cast<const u32x4*>(lds + L_XN + row * 4);
#pragma unroll
      for (int bb = 0; bb < 4; ++bb) {
        const unsigned xl = xq[bb], xh = xq[bb] << 16;
#pragma unroll
        for (int k = 0; k < 4; ++k) {
          qa[bb * 8 + 2 * k] = dot2(wqr[j][k], xl, qa[bb * 8 + 2 * k]);
          qa[bb * 8 + 2 * k + 1] = dot2(wqr[j][k], xh, qa[bb * 8 + 2 * k + 1]);
        }
      }
    }
#pragma unroll
    for (int k = 0; k < 16; ++k) qa[k] = swap32_add(qa[k], qa[k + 16]);
#pragma unroll
    for (int k = 0; k < 8; ++k) qa[k] = swap16_add(qa[k], qa[k + 8]);
    f32x4 qs;
#pragma unroll
    for (int k = 0; k < 4; ++k) {
      const float t1 = qa[k] + dpp<ROR8>(qa[k]), t2 = qa[k + 4] + dpp<ROR8>(qa[k + 4]);
      qs[k] = (lane & 8) ? t2 : t1;
    }
    *reinterpret_cast<f32x4*>(ldf + L_QP + w * 256 + (lane >> 4) * 64 + (lane & 7) * 8 + ((lane >> 3) & 1) * 4) = qs;
  }
  LDS_BARRIER();
  STAMP(2);
  if (w < 2) {
    float q0 = 0.f, q1 = 0.f;
#pragma unroll
    for (int ww = 0; ww < NWAVE; ++ww) {
      const float2 t = *reinterpret_cast<const float2*>(ldf + L_QP + ww * 256 + tid * 2);
      q0 += t.x; q1 += t.y;
    }
    const u32x4 gq = {tag, __float_as_uint(q0), tag, __float_as_uint(q1)};
    __builtin_amdgcn_raw_buffer_store_b128(gq, rs, WS_QG + (((tid >> 5) * 4 + ks) * QN + cb * 64 + (tid & 31) * 2) * 8, 0, AUX_SC1);
    const int qo = WS_QG + ((b * 4) * QN + kvh * 256 + tid * 2) * 8;
    u32x4 gv[4];
    bool ok = false;
#pragma unroll 1
    for (unsigned spins = 0; spins < SPIN_CAP; ++spins) {
      if (!ok) {
#pragma unroll
        for (int s = 0; s < 4; ++s) gv[s] = __builtin_amdgcn_raw_buffer_load_b128(rs, qo, s * QN * 8, AUX_SC1);
        ok = true;
#pragma unroll
        for (int s = 0; s < 4; ++s) ok &= (gv[s][0] == tag) & (gv[s][2] == tag);
      }
      if (__all(ok)) break;
      asm volatile("" ::: "memory");
    }
    q0 = (__uint_as_float(gv[0][1]) + __uint_as_float(gv[1][1])) + (__uint_as_float(gv[2][1]) + __uint_as_float(gv[3][1]));
    q1 = (__uint_as_float(gv[0][3]) + __uint_as_float(gv[1][3])) + (__uint_as_float(gv[2][3]) + __uint_as_float(gv[3][3]));
    lds[L_QB + tid] = cvtpk(q0, q1);
    asm volatile("" ::: "memory");
    if (lane == 0) __hip_atomic_store(lds + L_FLAG + w, 1u, __ATOMIC_RELAXED, __HIP_MEMORY_SCOPE_WORKGROUP);
    asm volatile("" ::: "memory");
  }
  auto q_ready = [&]() {
    const unsigned long long f = __hip_atomic_load(reinterpret_cast<unsigned long long*>(lds + L_FLAG), __ATOMIC_RELAXED, __HIP_MEMORY_SCOPE_WORKGROUP);
    return __builtin_amdgcn_readfirstlane((unsigned)f & (unsigned)(f >> 32)) != 0;
  };
  bool ready = false;
#define CHUNK(dst, LD, pg, half) do { SB(); _Pragma("unroll") for (int t = 4 * (half); t < 4 * (half) + 4; ++t) dst[t] = LD(pg, t); SB(); \
    if (!ready) { asm volatile("s_waitcnt vmcnt(4)" ::: "memory"); ready = q_ready(); } SB(); } while (0)
  CHUNK(vb0, LDV, 0, 0);
  CHUNK(vb0, LDV, 0, 1);
  CHUNK(kb1, LDK, 1, 0);
  CHUNK(kb1, LDK, 1, 1);
  CHUNK(vb1, LDV, 1, 0);
#pragma unroll
  for (int t = 4; t < 8; ++t) vb1[t] = LDV(1, t);
  SB();
  u32x4 mw[4], wt[3];
#pragma unroll
  for (int j = 0; j < 3; ++j) {
    const unsigned short* src = j == 0 ? w1 : (j == 1 ? b1 : w2);
    mw[j] = reinterpret_cast<const u32x4*>(src)[72 * w + lane];
  }
  {
    const int a = lane >> 3;
    const unsigned short* src = a == 0 ? w1 : (a == 1 ? b1 : w2);
    mw[3] = reinterpret_cast<const u32x4*>(src)[72 * w + 64 + (lane & 7)];
  }
#pragma unroll
  for (int j = 0; j < 3; ++j) {
    const int ch = tid + NTHR * j;
    const int row = (ch * 7282) >> 16;
    const int vo = row * (DM * 2) + (ch - row * 9) * 16;
    wt[j] = __builtin_amdgcn_raw_buffer_load_b128(rwo, (j < 2 || tid < 128) ? vo : 0x7fffffff, (h1 * DH * DM + cbk * CB) * 2, AUX_NT);
  }
  SB();
  while (!ready) { __builtin_amdgcn_s_sleep(1); ready = q_ready(); }
  asm volatile("" ::: "memory");
  const u32x4 qh0 = *reinterpret_cast<const u32x4*>(lds + L_QB + c * 4);
  const u32x4 qh1 = *reinterpret_cast<const u32x4*>(lds + L_QB + 64 + c * 4);
  float hv[NOUT];
  float ya[16];
#pragma unroll
  for (int j = 0; j < 16; ++j) ya[j] = 0.f;
#pragma unroll
  for (int j = 0; j < NOUT; ++j) hv[j] = __builtin_bit_cast(float, __builtin_amdgcn_readfirstlane(lds[L_H + col0 + j]));
  const unsigned short* mwl = reinterpret_cast<const unsigned short*>(lds + L_MW + w * MW_W);

  auto mlp_slice = [&](int k) {
    const int f = k * 64 + lane;
    const float w1f = bf2f(mwl[f]), b1f = bf2f(mwl[576 + f]), w2f = bf2f(mwl[2 * 576 + f]);
    constexpr float C1 = -2.0f * 0.7978845608028654f * LOG2E, C2 = C1 * 0.044715f;
#pragma unroll
    for (int j = 0; j < NOUT; ++j) {
      const float uu = fmaf(hv[j], w1f, b1f);
      const float e = ex2(uu * fmaf(uu * uu, C2, C1));
      ya[j] = fmaf(uu * w2f, rcp(1.0f + e), ya[j]);
    }
  };

  float m = -INFINITY, l = 0.f;
  float a0[8], a1[8];
#pragma unroll
  for (int k = 0; k < 8; ++k) { a0[k] = 0.f; a1[k] = 0.f; }

#define ISSUE(N, so, first) do { if (!(first) || (N) >= 8) { \
      _Pragma("unroll") for (int t = 0; t < 4; ++t) { \
        const int o_ = ((N) < 8 ? (so) : so_n) + (((N) >> 2) & 1) * PSTR + ((N) & 1) * 4096; \
        if (((N) & 6) == 0) kb0[4 * ((N) & 1) + t] = __builtin_amdgcn_raw_buffer_load_b128(rk, lo16 + t * 1024, o_, AUX_NT); \
        if (((N) & 6) == 2) vb0[4 * ((N) & 1) + t] = __builtin_amdgcn_raw_buffer_load_b128(rv, lo16 + t * 1024, o_, AUX_NT); \
        if (((N) & 6) == 4) kb1[4 * ((N) & 1) + t] = __builtin_amdgcn_raw_buffer_load_b128(rk, lo16 + t * 1024, o_, AUX_NT); \
        if (((N) & 6) == 6) vb1[4 * ((N) & 1) + t] = __builtin_amdgcn_raw_buffer_load_b128(rv, lo16 + t * 1024, o_, AUX_NT); \
        SB(); } } } while (0)
#define QK_HALF(kb, hf) _Pragma("unroll") for (int t = 4 * (hf); t < 4 * (hf) + 4; ++t) { \
      float d0 = 0.f, d1 = 0.f; \
      _Pragma("unroll") for (int k = 0; k < 4; ++k) { d0 = dot2(kb[t][k], qh0[k], d0); d1 = dot2(kb[t][k], qh1[k], d1); } \
      xs[2 * t] = d0; xs[2 * t + 1] = d1; }
#define TR_STEP(HALF, CTRL, BIT) _Pragma("unroll") for (int h = 0; h < (HALF); ++h) { \
      const float t1_ = xs[h] + dpp<CTRL>(xs[h]), t2_ = xs[h + (HALF)] + dpp<CTRL>(xs[h + (HALF)]); \
      xs[h] = (c & (BIT)) ? t2_ : t1_; }
#define PV_STEP(vb, t) { const float p0 = dpp<BCAST + 2 * (t)>(p), p1 = dpp<BCAST + 2 * (t) + 1>(p); \
      _Pragma("unroll") for (int k = 0; k < 4; ++k) { const float lo = bf_lo(vb[t][k]), hi = bf_hi(vb[t][k]); \
        a0[2 * k] = fmaf(p0, lo, a0[2 * k]); a0[2 * k + 1] = fmaf(p0, hi, a0[2 * k + 1]); \
        a1[2 * k] = fmaf(p1, lo, a1[2 * k]); a1[2 * k + 1] = fmaf(p1, hi, a1[2 * k + 1]); } }
#define PAGE_STEP(kb, vb, S, so, first) do { \
    float xs[16]; \
    QK_HALF(kb, 0) \
    PIN8(xs); SB(); \
    ISSUE((S) + DEPTH, so, first); \
    QK_HALF(kb, 1) \
    PIN8((xs + 8)); SB(); \
    ISSUE((S) + 1 + DEPTH, so, first); \
    TR_STEP(8, ROR8, 8) TR_STEP(4, XOR7, 4) TR_STEP(2, XOR2, 2) TR_STEP(1, XOR1, 1) \
    const float sc = xs[0] * SC2; \
    float pm = fmaxf(sc, dpp<XOR2>(sc)); \
    pm = fmaxf(pm, dpp<ROR4>(pm)); \
    pm = fmaxf(pm, dpp<ROR8>(pm)); \
    const float mn = fmaxf(m, pm); \
    const float alpha = ex2(m - mn), p = ex2(sc - mn); \
    l = fmaf(l, alpha, p); \
    m = mn; \
    const float al0 = dpp<QP0>(alpha), al1 = dpp<QP1>(alpha); \
    _Pragma("unroll") for (int k = 0; k < 8; ++k) { a0[k] *= al0; a1[k] *= al1; } \
    PV_STEP(vb, 0) PV_STEP(vb, 1) PV_STEP(vb, 2) PV_STEP(vb, 3) \
    PIN8(a0); PIN8(a1); SB(); \
    ISSUE((S) + 2 + DEPTH, so, first); \
    PV_STEP(vb, 4) PV_STEP(vb, 5) PV_STEP(vb, 6) PV_STEP(vb, 7) \
    PIN8(a0); PIN8(a1); SB(); \
    ISSUE((S) + 3 + DEPTH, so, first); \
  } while (0)

  STAMP(3);
  int so_n = w * PBYTES + ((rot + 2) & 7) * PSTR;
  PAGE_STEP(kb0, vb0, 0, wpo, true);
  STAMP(4);
  PAGE_STEP(kb1, vb1, 4, wpo, true);
  {
    unsigned* mwd = lds + L_MW + w * MW_W;
#pragma unroll
    for (int j = 0; j < 3; ++j) *reinterpret_cast<u32x4*>(mwd + j * 288 + lane * 4) = mw[j];
    if (lane < 24) *reinterpret_cast<u32x4*>(mwd + (lane >> 3) * 288 + 256 + (lane & 7) * 4) = mw[3];
#pragma unroll
    for (int j = 0; j < 2; ++j) *reinterpret_cast<u32x4*>(lds + L_WT + (tid + NTHR * j) * 4) = wt[j];
    if (tid < 128) *reinterpret_cast<u32x4*>(lds + L_WT + (tid + NTHR * 2) * 4) = wt[2];
    asm volatile("" ::: "memory");
    mlp_slice(0);
    mlp_slice(1);
    mlp_slice(2);
  }
  SB();
  STAMP(5);
#pragma unroll 1
  for (int i = 2; i < NPG; i += 2) {
    const int so = so_n;
    so_n = i + 2 < NPG ? w * PBYTES + ((rot + i + 2) & 7) * PSTR : 0x40000000;
    mlp_slice(i + 1);
    SB();
    PAGE_STEP(kb0, vb0, 0, so, false);
    mlp_slice(i + 2);
    SB();
    PAGE_STEP(kb1, vb1, 4, so, false);
  }
  STAMP(7);

  {
    const float mw_ = xrow_max(m);
    const float sc_ = ex2(m - mw_);
    float lr = l * sc_;
    lr += dpp<XOR2>(lr);
    lr += dpp<ROR4>(lr);
    lr += dpp<ROR8>(lr);
    lr = xrow_sum(lr);
    const float s0 = dpp<QP0>(sc_), s1 = dpp<QP1>(sc_);
    float t8[8];
#pragma unroll
    for (int k = 0; k < 8; ++k) t8[k] = swap32_add(a0[k] * s0, a1[k] * s1);
    f32x4 t4;
#pragma unroll
    for (int k = 0; k < 4; ++k) t4[k] = swap16_add(t8[k], t8[k + 4]);
    float* rec = ldf + L_MW + w * MW_W;
    *reinterpret_cast<f32x4*>(rec + (lane >> 5) * 128 + c * 8 + (r & 1) * 4) = t4;
    if (lane < 2) { rec[256 + lane * 2] = mw_; rec[257 + lane * 2] = lr; }
  }
  LDS_BARRIER();
  STAMP(8);
  if (w < 2) {
    float mm[NWAVE], ll[NWAVE];
    float M = -INFINITY;
#pragma unroll
    for (int ww = 0; ww < NWAVE; ++ww) {
      const float2 t = *reinterpret_cast<const float2*>(ldf + L_MW + ww * MW_W + 256 + w * 2);
      mm[ww] = t.x; ll[ww] = t.y; M = fmaxf(M, t.x);
    }
    float O0 = 0.f, O1 = 0.f, L = 0.f;
#pragma unroll
    for (int ww = 0; ww < NWAVE; ++ww) {
      const float wgt = ex2(mm[ww] - M);
      L = fmaf(wgt, ll[ww], L);
      const float2 t = *reinterpret_cast<const float2*>(ldf + L_MW + ww * MW_W + w * 128 + lane * 2);
      O0 = fmaf(wgt, t.x, O0); O1 = fmaf(wgt, t.y, O1);
    }
    const int ro = WS_OG + (prod * 2 + w) * OREC;
    const u32x4 go = {tag, __float_as_uint(O0), tag, __float_as_uint(O1)};
    __builtin_amdgcn_raw_buffer_store_b128(go, rs, lo16, ro, AUX_SC1);
    const u32x4 gm = {tag, __float_as_uint(M), tag, __float_as_uint(L)};
    __builtin_amdgcn_raw_buffer_store_b128(gm, rs, lane == 0 ? 1024 : 0x7fffffff, ro, AUX_SC1);
  }
  STAMP(9);
  {
    const int ro = WS_OG + (((w >> 1) * 64 + (h1 >> 1) * 4) * 2 + (h1 & 1)) * OREC;
    const int vo = r * 2 * OREC + ((w & 1) * 64 + c * 4) * 8;
    u32x4 g0, g1, gm;
    g0 = __builtin_amdgcn_raw_buffer_load_b128(rs, vo, ro, AUX_SC1);
    g1 = __builtin_amdgcn_raw_buffer_load_b128(rs, vo + 16, ro, AUX_SC1);
    gm = __builtin_amdgcn_raw_buffer_load_b128(rs, r * 2 * OREC + 1024, ro, AUX_SC1);
    SB();
    const float ysum = wave_treduce16(ya, lane);
    if ((lane & 3) == 0) ldf[L_Y + w * 16 + (lane >> 2)] = ysum;
    u32x4 bfr[4];
    if (w < 5) {
      const unsigned short* wts = reinterpret_cast<const unsigned short*>(lds + L_WT) + (r * 8) * CB + w * 16 + c;
#pragma unroll
      for (int kk = 0; kk < 4; ++kk)
#pragma unroll
        for (int j = 0; j < 4; ++j)
          bfr[kk][j] = (unsigned)wts[(kk * 32 + 2 * j) * CB] | ((unsigned)wts[(kk * 32 + 2 * j + 1) * CB] << 16);
    }
    SB();
    bool ok = (g0[0] == tag) & (g0[2] == tag) & (g1[0] == tag) & (g1[2] == tag) & (gm[0] == tag) & (gm[2] == tag);
#pragma unroll 1
    for (unsigned spins = 0; spins < SPIN_CAP; ++spins) {
      const unsigned long long miss = __ballot(!ok);
      if (miss == 0) break;
      if (__popcll(miss) > BO_T1) __builtin_amdgcn_s_sleep(BO_S);
      if (!ok) {
        g0 = __builtin_amdgcn_raw_buffer_load_b128(rs, vo, ro, AUX_SC1);
        g1 = __builtin_amdgcn_raw_buffer_load_b128(rs, vo + 16, ro, AUX_SC1);
        gm = __builtin_amdgcn_raw_buffer_load_b128(rs, r * 2 * OREC + 1024, ro, AUX_SC1);
        ok = (g0[0] == tag) & (g0[2] == tag) & (g1[0] == tag) & (g1[2] == tag) & (gm[0] == tag) & (gm[2] == tag);
      }
      asm volatile("" ::: "memory");
    }
    STAMP(10);
    const float Ms = __uint_as_float(gm[1]);
    const float wgt = ex2(Ms - xrow_max(Ms));
    const float Ls = xrow_sum(wgt * __uint_as_float(gm[3]));
    const float t0 = swap32_add(wgt * __uint_as_float(g0[1]), wgt * __uint_as_float(g1[1]));
    const float t1 = swap32_add(wgt * __uint_as_float(g0[3]), wgt * __uint_as_float(g1[3]));
    const float ov = swap16_add(t0, t1) / Ls;
    reinterpret_cast<unsigned short*>(lds + L_O)[(w >> 1) * DH + (w & 1) * 64 + c * 4 + r] = (unsigned short)(cvtpk(ov, 0.f) & 0xffffu);
    LDS_BARRIER();
    if (w < 5) {
      f32x4 acc0 = {0.f, 0.f, 0.f, 0.f}, acc1 = {0.f, 0.f, 0.f, 0.f};
      u32x4 afr[4];
#pragma unroll
      for (int kk = 0; kk < 4; ++kk) afr[kk] = *reinterpret_cast<const u32x4*>(lds + L_O + (c & 3) * (DH / 2) + kk * 16 + r * 4);
      acc0 = __builtin_amdgcn_mfma_f32_16x16x32_bf16(__builtin_bit_cast(bf16x8, afr[0]), __builtin_bit_cast(bf16x8, bfr[0]), acc0, 0, 0, 0);
      acc1 = __builtin_amdgcn_mfma_f32_16x16x32_bf16(__builtin_bit_cast(bf16x8, afr[1]), __builtin_bit_cast(bf16x8, bfr[1]), acc1, 0, 0, 0);
      acc0 = __builtin_amdgcn_mfma_f32_16x16x32_bf16(__builtin_bit_cast(bf16x8, afr[2]), __builtin_bit_cast(bf16x8, bfr[2]), acc0, 0, 0, 0);
      acc1 = __builtin_amdgcn_mfma_f32_16x16x32_bf16(__builtin_bit_cast(bf16x8, afr[3]), __builtin_bit_cast(bf16x8, bfr[3]), acc1, 0, 0, 0);
      const int so = (lane < 16 && w * 16 + lane < CB) ? lane * HQ * 8 : 0x7fffffff;
#pragma unroll
      for (int i = 0; i < 4; ++i) {
        const u32x2 gs = {tag, __float_as_uint(acc0[i] + acc1[i])};
        __builtin_amdgcn_raw_buffer_store_b64(gs, rs, so, WS_SG + ((i * DM + cbk * CB + w * 16) * HQ + h1) * 8, AUX_SC1);
      }
      STAMP(11);
    } else {
      const int j = (w - 5) * 4 + r;
      const bool val = j < NOUT;
      const int so = WS_SG + ((b * DM + col0 + (w - 5) * 4) * HQ) * 8;
      const int vo2 = val ? lo16 : 0x7fffffff;
      u32x4 gs = __builtin_amdgcn_raw_buffer_load_b128(rs, vo2, so, AUX_SC1);
      SB();
      const int jj = val ? j : 0;
      float y = ldf[L_FLAG + 2];
#pragma unroll
      for (int ww = 0; ww < NWAVE; ++ww) y += ldf[L_Y + ww * 16 + jj];
      const float g = y / (1.0f + __expf(-y));
      const float xo = ldf[L_X + col0 + jj];
      SB();
      bool ok2 = !val | ((gs[0] == tag) & (gs[2] == tag));
#pragma unroll 1
    for (unsigned spins = 0; spins < SPIN_CAP; ++spins) {
        const unsigned long long miss = __ballot(!ok2);
        if (miss == 0) break;
        if (__popcll(miss) > BO_T2) __builtin_amdgcn_s_sleep(BO_S);
        if (!ok2) {
          gs = __builtin_amdgcn_raw_buffer_load_b128(rs, vo2, so, AUX_SC1);
          ok2 = (gs[0] == tag) & (gs[2] == tag);
        }
        asm volatile("" ::: "memory");
      }
      const float s = row_sum(__uint_as_float(gs[1]) + __uint_as_float(gs[3]));
      if (val && c == 0) out[b * DM + col0 + j] = (unsigned short)(cvtpk(fmaf(s, g, xo), 0.f) & 0xffffu);
      if (bid == 0 && w == 5 && lane == 0) __hip_atomic_store(reinterpret_cast<unsigned*>(ws + WS_EPOCH), tag, RLX_AGENT);
      STAMP(12);
    }
  }
}

}

extern "C" void kernel_launch(void* const* d_in, const int* in_sizes, int n_in, void* d_out, int out_size, void* d_ws, size_t ws_size,
                              hipStream_t stream) {
  (void)in_sizes; (void)n_in; (void)out_size; (void)ws_size;
  fused_decode_kernel<<<NWG, NTHR, 0, stream>>>(
      (const unsigned short*)d_in[0], (const unsigned short*)d_in[1], (const unsigned short*)d_in[2], (const unsigned short*)d_in[3],
      (const unsigned short*)d_in[4], (const unsigned short*)d_in[6], (const unsigned short*)d_in[7], (const unsigned short*)d_in[8],
      (const unsigned short*)d_in[9], (unsigned short*)d_out, (unsigned char*)d_ws);
}
```

```cpp
#include <hip/hip_runtime.h>
#include <hip/hip_bf16.h>
#include <stdint.h>

#define FAIR 0
#define DEPTH 2
#define BO_T1 16
#define BO_T2 8
#define BO_S 16
#define AUX_K 19
#define AUX_V 19
#define MAPV 3
#define ROTV 0

namespace {

constexpr int NB = 4, DM = 576, HQ = 32, HKV = 16, SK = 8192, DH = 128, FF = 4608;
constexpr int NWG = 256, NTHR = 512, NWAVE = 8;
constexpr int NSPLIT = 4;
constexpr int KEYS_WG = SK / NSPLIT;
constexpr int PAGE = 32;
constexpr int NPG = KEYS_WG / PAGE / NWAVE;
constexpr int QN = HQ * DH;
constexpr float LOG2E = 1.4426950408889634f;
constexpr float SC2 = 0.08838834764831845f * LOG2E;
constexpr float EPS = 1e-5f;
constexpr int CB = 72;
constexpr int NOUT = 9;

constexpr int WS_EPOCH = 0;
constexpr int WS_QG = 4096;
constexpr int WS_OG = WS_QG + NB * 4 * QN * 8;
constexpr int OREC = 1152;
constexpr int WS_SG = WS_OG + NWG * 2 * OREC;
constexpr int WS_BYTES = WS_SG + NB * DM * HQ * 8;
constexpr unsigned TAG_STEP = 0x9E3779B1u;
constexpr unsigned SPIN_CAP = 1u << 20;

constexpr int L_XN = 0;
constexpr int L_QP = L_XN + DM * 4;
constexpr int L_QB = L_QP + NWAVE * 256;
constexpr int L_FLAG = L_QB + 128;
constexpr int L_H = L_FLAG + 4;
constexpr int L_X = L_H + DM;
constexpr int L_MW = L_X + DM;
constexpr int MW_W = 3 * 288;
constexpr int L_WT = L_MW + NWAVE * MW_W;
constexpr int L_O = L_WT + DH * CB / 2 + 8;
constexpr int L_Y = L_O + NB * DH / 2;
constexpr int L_WORDS = L_Y + NWAVE * 16;
static_assert(L_WORDS * 4 <= 163840, "LDS");
static_assert((L_QP % 4 | L_QB % 4 | L_MW % 4 | L_WT % 4 | L_O % 4 | L_FLAG % 2) == 0, "align");

typedef unsigned u32x4 __attribute__((ext_vector_type(4)));
typedef unsigned u32x2 __attribute__((ext_vector_type(2)));
typedef float f32x4 __attribute__((ext_vector_type(4)));
typedef __bf16 bf16x2 __attribute__((ext_vector_type(2)));
typedef __bf16 bf16x8 __attribute__((ext_vector_type(8)));
#define RLX_AGENT __ATOMIC_RELAXED, __HIP_MEMORY_SCOPE_AGENT
constexpr int AUX_SC1 = 16, AUX_NT = 2;

__device__ __forceinline__ float dot2(unsigned a, unsigned b, float acc) {
  return __builtin_amdgcn_fdot2_f32_bf16(__builtin_bit_cast(bf16x2, a), __builtin_bit_cast(bf16x2, b), acc, false);
}
__device__ __forceinline__ unsigned cvtpk(float lo, float hi) {
  bf16x2 r = {(__bf16)lo, (__bf16)hi};
  return __builtin_bit_cast(unsigned, r);
}
__device__ __forceinline__ float bf_lo(unsigned v) { return __builtin_bit_cast(float, v << 16); }
__device__ __forceinline__ float bf_hi(unsigned v) { return __builtin_bit_cast(float, v & 0xffff0000u); }
__device__ __forceinline__ float bf2f(unsigned short v) { return __builtin_bit_cast(float, (unsigned)v << 16); }
template <int CTRL> __device__ __forceinline__ float dpp(float x) {
  return __builtin_bit_cast(float, __builtin_amdgcn_mov_dpp(__builtin_bit_cast(int, x), CTRL, 0xf, 0xf, true));
}
constexpr int XOR1 = 0xB1, XOR2 = 0x4E, XOR7 = 0x141, ROR8 = 0x128, ROR4 = 0x124, BCAST = 0x150;
constexpr int QP0 = 0x00, QP1 = 0x55;
__device__ __forceinline__ float ex2(float x) { return __builtin_amdgcn_exp2f(x); }
__device__ __forceinline__ float rcp(float x) { return __builtin_amdgcn_rcpf(x); }

__device__ __forceinline__ float swap32_add(float a, float b) {
  auto s = __builtin_amdgcn_permlane32_swap(__float_as_uint(a), __float_as_uint(b), false, false);
  return __uint_as_float(s[0]) + __uint_as_float(s[1]);
}
__device__ __forceinline__ float swap16_add(float a, float b) {
  auto s = __builtin_amdgcn_permlane16_swap(__float_as_uint(a), __float_as_uint(b), false, false);
  return __uint_as_float(s[0]) + __uint_as_float(s[1]);
}
__device__ __forceinline__ float xrow_max(float x) {
  auto s = __builtin_amdgcn_permlane16_swap(__float_as_uint(x), __float_as_uint(x), false, false);
  x = fmaxf(__uint_as_float(s[0]), __uint_as_float(s[1]));
  auto t = __builtin_amdgcn_permlane32_swap(__float_as_uint(x), __float_as_uint(x), false, false);
  return fmaxf(__uint_as_float(t[0]), __uint_as_float(t[1]));
}
__device__ __forceinline__ float xrow_sum(float x) {
  x = swap16_add(x, x);
  return swap32_add(x, x);
}
__device__ __forceinline__ float row_sum(float x) {
  x += dpp<XOR1>(x);
  x += dpp<XOR2>(x);
  x += dpp<XOR7>(x);
  x += dpp<ROR8>(x);
  return x;
}
__device__ __forceinline__ float wave_sum(float x) { return xrow_sum(row_sum(x)); }
__device__ __forceinline__ float wave_treduce16(float (&v)[16], int lane) {
#pragma unroll
  for (int j = 0; j < 8; ++j) v[j] = swap32_add(v[j], v[j + 8]);
#pragma unroll
  for (int j = 0; j < 4; ++j) v[j] = swap16_add(v[j], v[j + 4]);
#pragma unroll
  for (int j = 0; j < 2; ++j) {
    const float a = v[j] + dpp<ROR8>(v[j]), b = v[j + 2] + dpp<ROR8>(v[j + 2]);
    v[j] = (lane & 8) ? b : a;
  }
  {
    const float a = v[0] + dpp<XOR7>(v[0]), b = v[1] + dpp<XOR7>(v[1]);
    v[0] = (lane & 4) ? b : a;
  }
  float r = v[0];
  r += dpp<XOR2>(r);
  r += dpp<XOR1>(r);
  return r;
}

#define SB() __builtin_amdgcn_sched_barrier(0)
#define STAMP(n)
#define PIN8(a) asm volatile("" : "+v"(a[0]), "+v"(a[1]), "+v"(a[2]), "+v"(a[3]), "+v"(a[4]), "+v"(a[5]), "+v"(a[6]), "+v"(a[7]) : : "memory")
#define LDS_BARRIER() do { asm volatile("s_waitcnt lgkmcnt(0)" ::: "memory"); __builtin_amdgcn_s_barrier(); asm volatile("" ::: "memory"); } while (0)

__global__ void __launch_bounds__(NTHR, 2)
fused_decode_kernel(const unsigned short* __restrict__ x, const unsigned short* __restrict__ kc, const unsigned short* __restrict__ vc,
                    const unsigned short* __restrict__ wq, const unsigned short* __restrict__ wo, const unsigned short* __restrict__ w1,
                    const unsigned short* __restrict__ b1, const unsigned short* __restrict__ w2, const unsigned short* __restrict__ b2,
                    unsigned short* __restrict__ out, unsigned char* ws) {
  __shared__ __attribute__((aligned(16))) unsigned lds[L_WORDS];
  float* ldf = reinterpret_cast<float*>(lds);
  const int tid = threadIdx.x, lane = tid & 63, w = __builtin_amdgcn_readfirstlane(tid >> 6);
  const int r = lane >> 4, c = lane & 15;
  const int bid = blockIdx.x, xcd = bid & 7, ii = bid >> 3;
  const int kvh = ii & 15, b = (xcd >> 1) & 3, sp = ((xcd & 1) << 1) | (ii >> 4);
  const int rot = 0;
  const int prod = b * 64 + kvh * 4 + sp;
  const int col0 = (prod & 63) * NOUT;
  const int h1 = 4 * xcd + (ii >> 3), cbk = ii & 7;

  STAMP(0);
  const unsigned tag = *reinterpret_cast<const __attribute__((address_space(4))) unsigned*>(reinterpret_cast<uintptr_t>(ws + WS_EPOCH)) + TAG_STEP;
  const int lnb = w < 4 ? w : b;
  const __amdgpu_buffer_rsrc_t rx = __builtin_amdgcn_make_buffer_rsrc((void*)x, 0, NB * DM * 2, 0x00020000);
  const __amdgpu_buffer_rsrc_t rb2 = __builtin_amdgcn_make_buffer_rsrc((void*)b2, 0, 2, 0x00020000);
  unsigned short xr[9];
#pragma unroll
  for (int j = 0; j < 9; ++j) xr[j] = __builtin_amdgcn_raw_buffer_load_b16(rx, lane * 2 + j * 128, w <= 4 ? lnb * DM * 2 : 0x10000, 0);
  const unsigned short b2r = __builtin_amdgcn_raw_buffer_load_b16(rb2, 0, w == 4 ? 0 : 0x10000, 0);
  const size_t kvbase = ((size_t)(b * HKV + kvh) * SK + (size_t)sp * KEYS_WG) * DH;
  const __amdgpu_buffer_rsrc_t rk = __builtin_amdgcn_make_buffer_rsrc((void*)(kc + kvbase), 0, KEYS_WG * DH * 2, 0x00020000);
  const __amdgpu_buffer_rsrc_t rv = __builtin_amdgcn_make_buffer_rsrc((void*)(vc + kvbase), 0, KEYS_WG * DH * 2, 0x00020000);
  const __amdgpu_buffer_rsrc_t rwq = __builtin_amdgcn_make_buffer_rsrc((void*)wq, 0, DM * QN * 2, 0x00020000);
  const __amdgpu_buffer_rsrc_t rwo = __builtin_amdgcn_make_buffer_rsrc((void*)wo, 0, DM * QN * 2, 0x00020000);
  const __amdgpu_buffer_rsrc_t rs = __builtin_amdgcn_make_buffer_rsrc((void*)ws, 0, WS_BYTES, 0x00020000);
  constexpr int PBYTES = PAGE * DH * 2;
  constexpr int PSTR = NWAVE * PBYTES;
  const int lo16 = lane * 16;
  const int wpo = w * PBYTES + rot * PSTR;
#define LDK(i, t) __builtin_amdgcn_raw_buffer_load_b128(rk, lo16 + ((t) & 3) * 1024, wpo + (i) * PSTR + ((t) >> 2) * 4096, AUX_K)
#define LDV(i, t) __builtin_amdgcn_raw_buffer_load_b128(rv, lo16 + ((t) & 3) * 1024, wpo + (i) * PSTR + ((t) >> 2) * 4096, AUX_V)
  u32x4 kb0[8], vb0[8], kb1[8], vb1[8];
  const int cb = bid & 63, ks = bid >> 6;
  const int qrow = ks * 144 + w * 18 + (lane >> 3);
  u32x4 wqr[3];
  {
    const int wqv = (lane >> 3) * QN * 2 + (lane & 7) * 16;
    const int wqs = ((ks * 144 + w * 18) * QN + cb * 64) * 2;
    wqr[0] = __builtin_amdgcn_raw_buffer_load_b128(rwq, wqv, wqs, AUX_NT);
    wqr[1] = __builtin_amdgcn_raw_buffer_load_b128(rwq, wqv, wqs + 8 * QN * 2, AUX_NT);
    wqr[2] = __builtin_amdgcn_raw_buffer_load_b128(rwq, lane < 16 ? wqv : 0x7fffffff, wqs + 16 * QN * 2, AUX_NT);
  }
  SB();
  asm volatile("" ::: "memory");
  __builtin_amdgcn_s_barrier();
  asm volatile("" ::: "memory");
  SB();
#pragma unroll
  for (int t = 0; t < 8; ++t) { kb0[t] = LDK(0, t); SB(); }
  asm volatile("" :: "v"(xr[0]), "v"(xr[1]), "v"(xr[2]), "v"(xr[3]), "v"(xr[4]), "v"(xr[5]), "v"(xr[6]), "v"(xr[7]), "v"(xr[8]), "v"(b2r));
  SB();

  if (w <= 4) {
    float xv[9];
    float s = 0.f;
#pragma unroll
    for (int j = 0; j < 9; ++j) { xv[j] = bf2f(xr[j]); s += xv[j]; }
    if (w == 4) {
#pragma unroll
      for (int j = 0; j < 9; ++j) ldf[L_X + j * 64 + lane] = xv[j];
      ldf[L_FLAG + 2] = bf2f(b2r);
    }
    const float mean = wave_sum(s) * (1.0f / DM);
    float v = 0.f;
#pragma unroll
    for (int j = 0; j < 9; ++j) { xv[j] -= mean; v = fmaf(xv[j], xv[j], v); }
    const float rstd = 1.0f / sqrtf(wave_sum(v) * (1.0f / DM) + EPS);
    if (w < 4) {
#pragma unroll
      for (int j = 0; j < 9; ++j) lds[L_XN + (j * 64 + lane) * 4 + w] = cvtpk(xv[j] * rstd, 0.f) & 0xffffu;
    } else {
      float s2 = 0.f;
#pragma unroll
      for (int j = 0; j < 9; ++j) { xv[j] = bf_lo(cvtpk(xv[j] * rstd, 0.f)); s2 += xv[j]; }
      const float mean2 = wave_sum(s2) * (1.0f / DM);
      float v2 = 0.f;
#pragma unroll
      for (int j = 0; j < 9; ++j) { xv[j] -= mean2; v2 = fmaf(xv[j], xv[j], v2); }
      const float rstd2 = 1.0f / sqrtf(wave_sum(v2) * (1.0f / DM) + EPS);
#pragma unroll
      for (int j = 0; j < 9; ++j) ldf[L_H + j * 64 + lane] = xv[j] * rstd2;
    }
  } else if (w == 5) {
    if (lane < 2) lds[L_FLAG + lane] = 0u;
  }
  LDS_BARRIER();
  STAMP(1);
  {
    float qa[32];
#pragma unroll
    for (int k = 0; k < 32; ++k) qa[k] = 0.f;
#pragma unroll
    for (int j = 0; j < 3; ++j) {
      const int row = (j < 2) ? qrow + 8 * j : min(qrow + 16, DM - 1);
      const u32x4 xq = *reinterpret_cast<const u32x4*>(lds + L_XN + row * 4);
#pragma unroll
      for (int bb = 0; bb < 4; ++bb) {
        const unsigned xl = xq[bb], xh = xq[bb] << 16;
#pragma unroll
        for (int k = 0; k < 4; ++k) {
          qa[bb * 8 + 2 * k] = dot2(wqr[j][k], xl, qa[bb * 8 + 2 * k]);
          qa[bb * 8 + 2 * k + 1] = dot2(wqr[j][k], xh, qa[bb * 8 + 2 * k + 1]);
        }
      }
    }
#pragma unroll
    for (int k = 0; k < 16; ++k) qa[k] = swap32_add(qa[k], qa[k + 16]);
#pragma unroll
    for (int k = 0; k < 8; ++k) qa[k] = swap16_add(qa[k], qa[k + 8]);
    f32x4 qs;
#pragma unroll
    for (int k = 0; k < 4; ++k) {
      const float t1 = qa[k] + dpp<ROR8>(qa[k]), t2 = qa[k + 4] + dpp<ROR8>(qa[k + 4]);
      qs[k] = (lane & 8) ? t2 : t1;
    }
    *reinterpret_cast<f32x4*>(ldf + L_QP + w * 256 + (lane >> 4) * 64 + (lane & 7) * 8 + ((lane >> 3) & 1) * 4) = qs;
  }
  LDS_BARRIER();
  STAMP(2);
  if (w < 2) {
    float q0 = 0.f, q1 = 0.f;
#pragma unroll
    for (int ww = 0; ww < NWAVE; ++ww) {
      const float2 t = *reinterpret_cast<const float2*>(ldf + L_QP + ww * 256 + tid * 2);
      q0 += t.x; q1 += t.y;
    }
    const u32x4 gq = {tag, __float_as_uint(q0), tag, __float_as_uint(q1)};
    __builtin_amdgcn_raw_buffer_store_b128(gq, rs, WS_QG + (((tid >> 5) * 4 + ks) * QN + cb * 64 + (tid & 31) * 2) * 8, 0, AUX_SC1);
    const int qo = WS_QG + ((b * 4) * QN + kvh * 256 + tid * 2) * 8;
    u32x4 gv[4];
    bool ok = false;
#pragma unroll 1
    for (unsigned spins = 0; spins < SPIN_CAP; ++spins) {
      if (!ok) {
#pragma unroll
        for (int s = 0; s < 4; ++s) gv[s] = __builtin_amdgcn_raw_buffer_load_b128(rs, qo, s * QN * 8, AUX_SC1);
        ok = true;
#pragma unroll
        for (int s = 0; s < 4; ++s) ok &= (gv[s][0] == tag) & (gv[s][2] == tag);
      }
      if (__all(ok)) break;
      asm volatile("" ::: "memory");
    }
    q0 = (__uint_as_float(gv[0][1]) + __uint_as_float(gv[1][1])) + (__uint_as_float(gv[2][1]) + __uint_as_float(gv[3][1]));
    q1 = (__uint_as_float(gv[0][3]) + __uint_as_float(gv[1][3])) + (__uint_as_float(gv[2][3]) + __uint_as_float(gv[3][3]));
    lds[L_QB + tid] = cvtpk(q0, q1);
    asm volatile("" ::: "memory");
    if (lane == 0) __hip_atomic_store(lds + L_FLAG + w, 1u, __ATOMIC_RELAXED, __HIP_MEMORY_SCOPE_WORKGROUP);
    asm volatile("" ::: "memory");
  }
  auto q_ready = [&]() {
    const unsigned long long f = __hip_atomic_load(reinterpret_cast<unsigned long long*>(lds + L_FLAG), __ATOMIC_RELAXED, __HIP_MEMORY_SCOPE_WORKGROUP);
    return __builtin_amdgcn_readfirstlane((unsigned)f & (unsigned)(f >> 32)) != 0;
  };
  bool ready = false;
#define CHUNK(dst, LD, pg, half) do { SB(); _Pragma("unroll") for (int t = 4 * (half); t < 4 * (half) + 4; ++t) dst[t] = LD(pg, t); SB(); \
    if (!ready) { asm volatile("s_waitcnt vmcnt(4)" ::: "memory"); ready = q_ready(); } SB(); } while (0)
  CHUNK(vb0, LDV, 0, 0);
  CHUNK(vb0, LDV, 0, 1);
  CHUNK(kb1, LDK, 1, 0);
  CHUNK(kb1, LDK, 1, 1);
  CHUNK(vb1, LDV, 1, 0);
#pragma unroll
  for (int t = 4; t < 8; ++t) vb1[t] = LDV(1, t);
  SB();
  u32x4 mw[4], wt[3];
#pragma unroll
  for (int j = 0; j < 3; ++j) {
    const unsigned short* src = j == 0 ? w1 : (j == 1 ? b1 : w2);
    mw[j] = reinterpret_cast<const u32x4*>(src)[72 * w + lane];
  }
  {
    const int a = lane >> 3;
    const unsigned short* src = a == 0 ? w1 : (a == 1 ? b1 : w2);
    mw[3] = reinterpret_cast<const u32x4*>(src)[72 * w + 64 + (lane & 7)];
  }
#pragma unroll
  for (int j = 0; j < 3; ++j) {
    const int ch = tid + NTHR * j;
    const int row = (ch * 7282) >> 16;
    const int vo = row * (DM * 2) + (ch - row * 9) * 16;
    wt[j] = __builtin_amdgcn_raw_buffer_load_b128(rwo, (j < 2 || tid < 128) ? vo : 0x7fffffff, (h1 * DH * DM + cbk * CB) * 2, AUX_NT);
  }
  SB();
  while (!ready) { __builtin_amdgcn_s_sleep(1); ready = q_ready(); }
  asm volatile("" ::: "memory");
  const u32x4 qh0 = *reinterpret_cast<const u32x4*>(lds + L_QB + c * 4);
  const u32x4 qh1 = *reinterpret_cast<const u32x4*>(lds + L_QB + 64 + c * 4);
  float hv[NOUT];
  float ya[16];
#pragma unroll
  for (int j = 0; j < 16; ++j) ya[j] = 0.f;
#pragma unroll
  for (int j = 0; j < NOUT; ++j) hv[j] = __builtin_bit_cast(float, __builtin_amdgcn_readfirstlane(lds[L_H + col0 + j]));
  const unsigned short* mwl = reinterpret_cast<const unsigned short*>(lds + L_MW + w * MW_W);

  auto mlp_slice = [&](int k) {
    const int f = k * 64 + lane;
    const float w1f = bf2f(mwl[f]), b1f = bf2f(mwl[576 + f]), w2f = bf2f(mwl[2 * 576 + f]);
    constexpr float C1 = -2.0f * 0.7978845608028654f * LOG2E, C2 = C1 * 0.044715f;
#pragma unroll
    for (int j = 0; j < NOUT; ++j) {
      const float uu = fmaf(hv[j], w1f, b1f);
      const float e = ex2(uu * fmaf(uu * uu, C2, C1));
      ya[j] = fmaf(uu * w2f, rcp(1.0f + e), ya[j]);
    }
  };

  float m = -INFINITY, l = 0.f;
  float a0[8], a1[8];
#pragma unroll
  for (int k = 0; k < 8; ++k) { a0[k] = 0.f; a1[k] = 0.f; }

#define ISSUE(N, so, first) do { if (!(first) || (N) >= 8) { \
      _Pragma("unroll") for (int t = 0; t < 4; ++t) { \
        const int o_ = ((N) < 8 ? (so) : so_n) + (((N) >> 2) & 1) * PSTR + ((N) & 1) * 4096; \
        if (((N) & 6) == 0) kb0[4 * ((N) & 1) + t] = __builtin_amdgcn_raw_buffer_load_b128(rk, lo16 + t * 1024, o_, AUX_K); \
        if (((N) & 6) == 2) vb0[4 * ((N) & 1) + t] = __builtin_amdgcn_raw_buffer_load_b128(rv, lo16 + t * 1024, o_, AUX_V); \
        if (((N) & 6) == 4) kb1[4 * ((N) & 1) + t] = __builtin_amdgcn_raw_buffer_load_b128(rk, lo16 + t * 1024, o_, AUX_K); \
        if (((N) & 6) == 6) vb1[4 * ((N) & 1) + t] = __builtin_amdgcn_raw_buffer_load_b128(rv, lo16 + t * 1024, o_, AUX_V); \
        SB(); } } } while (0)
#define QK_HALF(kb, hf) _Pragma("unroll") for (int t = 4 * (hf); t < 4 * (hf) + 4; ++t) { \
      float d0 = 0.f, d1 = 0.f; \
      _Pragma("unroll") for (int k = 0; k < 4; ++k) { d0 = dot2(kb[t][k], qh0[k], d0); d1 = dot2(kb[t][k], qh1[k], d1); } \
      xs[2 * t] = d0; xs[2 * t + 1] = d1; }
#define TR_STEP(HALF, CTRL, BIT) _Pragma("unroll") for (int h = 0; h < (HALF); ++h) { \
      const float t1_ = xs[h] + dpp<CTRL>(xs[h]), t2_ = xs[h + (HALF)] + dpp<CTRL>(xs[h + (HALF)]); \
      xs[h] = (c & (BIT)) ? t2_ : t1_; }
#define PV_STEP(vb, t) { const float p0 = dpp<BCAST + 2 * (t)>(p), p1 = dpp<BCAST + 2 * (t) + 1>(p); \
      _Pragma("unroll") for (int k = 0; k < 4; ++k) { const float lo = bf_lo(vb[t][k]), hi = bf_hi(vb[t][k]); \
        a0[2 * k] = fmaf(p0, lo, a0[2 * k]); a0[2 * k + 1] = fmaf(p0, hi, a0[2 * k + 1]); \
        a1[2 * k] = fmaf(p1, lo, a1[2 * k]); a1[2 * k + 1] = fmaf(p1, hi, a1[2 * k + 1]); } }
#define PAGE_STEP(kb, vb, S, so, first) do { \
    float xs[16]; \
    QK_HALF(kb, 0) \
    PIN8(xs); SB(); \
    ISSUE((S) + DEPTH, so, first); \
    QK_HALF(kb, 1) \
    PIN8((xs + 8)); SB(); \
    ISSUE((S) + 1 + DEPTH, so, first); \
    TR_STEP(8, ROR8, 8) TR_STEP(4, XOR7, 4) TR_STEP(2, XOR2, 2) TR_STEP(1, XOR1, 1) \
    const float sc = xs[0] * SC2; \
    float pm = fmaxf(sc, dpp<XOR2>(sc)); \
    pm = fmaxf(pm, dpp<ROR4>(pm)); \
    pm = fmaxf(pm, dpp<ROR8>(pm)); \
    const float mn = fmaxf(m, pm); \
    const float alpha = ex2(m - mn), p = ex2(sc - mn); \
    l = fmaf(l, alpha, p); \
    m = mn; \
    const float al0 = dpp<QP0>(alpha), al1 = dpp<QP1>(alpha); \
    _Pragma("unroll") for (int k = 0; k < 8; ++k) { a0[k] *= al0; a1[k] *= al1; } \
    PV_STEP(vb, 0) PV_STEP(vb, 1) PV_STEP(vb, 2) PV_STEP(vb, 3) \
    PIN8(a0); PIN8(a1); SB(); \
    ISSUE((S) + 2 + DEPTH, so, first); \
    PV_STEP(vb, 4) PV_STEP(vb, 5) PV_STEP(vb, 6) PV_STEP(vb, 7) \
    PIN8(a0); PIN8(a1); SB(); \
    ISSUE((S) + 3 + DEPTH, so, first); \
  } while (0)

  STAMP(3);
  int so_n = w * PBYTES + ((rot + 2) & 7) * PSTR;
  PAGE_STEP(kb0, vb0, 0, wpo, true);
  STAMP(4);
  PAGE_STEP(kb1, vb1, 4, wpo, true);
  {
    unsigned* mwd = lds + L_MW + w * MW_W;
#pragma unroll
    for (int j = 0; j < 3; ++j) *reinterpret_cast<u32x4*>(mwd + j * 288 + lane * 4) = mw[j];
    if (lane < 24) *reinterpret_cast<u32x4*>(mwd + (lane >> 3) * 288 + 256 + (lane & 7) * 4) = mw[3];
#pragma unroll
    for (int j = 0; j < 2; ++j) *reinterpret_cast<u32x4*>(lds + L_WT + (tid + NTHR * j) * 4) = wt[j];
    if (tid < 128) *reinterpret_cast<u32x4*>(lds + L_WT + (tid + NTHR * 2) * 4) = wt[2];
    asm volatile("" ::: "memory");
    mlp_slice(0);
    mlp_slice(1);
    mlp_slice(2);
  }
  SB();
  STAMP(5);
#pragma unroll 1
  for (int i = 2; i < NPG; i += 2) {
    const int so = so_n;
    so_n = i + 2 < NPG ? w * PBYTES + ((rot + i + 2) & 7) * PSTR : 0x40000000;
    mlp_slice(i + 1);
    SB();
    PAGE_STEP(kb0, vb0, 0, so, false);
    mlp_slice(i + 2);
    SB();
    PAGE_STEP(kb1, vb1, 4, so, false);
  }
  STAMP(7);

  {
    const float mw_ = xrow_max(m);
    const float sc_ = ex2(m - mw_);
    float lr = l * sc_;
    lr += dpp<XOR2>(lr);
    lr += dpp<ROR4>(lr);
    lr += dpp<ROR8>(lr);
    lr = xrow_sum(lr);
    const float s0 = dpp<QP0>(sc_), s1 = dpp<QP1>(sc_);
    float t8[8];
#pragma unroll
    for (int k = 0; k < 8; ++k) t8[k] = swap32_add(a0[k] * s0, a1[k] * s1);
    f32x4 t4;
#pragma unroll
    for (int k = 0; k < 4; ++k) t4[k] = swap16_add(t8[k], t8[k + 4]);
    float* rec = ldf + L_MW + w * MW_W;
    *reinterpret_cast<f32x4*>(rec + (lane >> 5) * 128 + c * 8 + (r & 1) * 4) = t4;
    if (lane < 2) { rec[256 + lane * 2] = mw_; rec[257 + lane * 2] = lr; }
  }
  LDS_BARRIER();
  STAMP(8);
  if (w < 2) {
    float mm[NWAVE], ll[NWAVE];
    float M = -INFINITY;
#pragma unroll
    for (int ww = 0; ww < NWAVE; ++ww) {
      const float2 t = *reinterpret_cast<const float2*>(ldf + L_MW + ww * MW_W + 256 + w * 2);
      mm[ww] = t.x; ll[ww] = t.y; M = fmaxf(M, t.x);
    }
    float O0 = 0.f, O1 = 0.f, L = 0.f;
#pragma unroll
    for (int ww = 0; ww < NWAVE; ++ww) {
      const float wgt = ex2(mm[ww] - M);
      L = fmaf(wgt, ll[ww], L);
      const float2 t = *reinterpret_cast<const float2*>(ldf + L_MW + ww * MW_W + w * 128 + lane * 2);
      O0 = fmaf(wgt, t.x, O0); O1 = fmaf(wgt, t.y, O1);
    }
    const int ro = WS_OG + (prod * 2 + w) * OREC;
    const u32x4 go = {tag, __float_as_uint(O0), tag, __float_as_uint(O1)};
    __builtin_amdgcn_raw_buffer_store_b128(go, rs, lo16, ro, AUX_SC1);
    const u32x4 gm = {tag, __float_as_uint(M), tag, __float_as_uint(L)};
    __builtin_amdgcn_raw_buffer_store_b128(gm, rs, lane == 0 ? 1024 : 0x7fffffff, ro, AUX_SC1);
  }
  STAMP(9);
  {
    const int ro = WS_OG + (((w >> 1) * 64 + (h1 >> 1) * 4) * 2 + (h1 & 1)) * OREC;
    const int vo = r * 2 * OREC + ((w & 1) * 64 + c * 4) * 8;
    u32x4 g0, g1, gm;
    g0 = __builtin_amdgcn_raw_buffer_load_b128(rs, vo, ro, AUX_SC1);
    g1 = __builtin_amdgcn_raw_buffer_load_b128(rs, vo + 16, ro, AUX_SC1);
    gm = __builtin_amdgcn_raw_buffer_load_b128(rs, r * 2 * OREC + 1024, ro, AUX_SC1);
    SB();
    const float ysum = wave_treduce16(ya, lane);
    if ((lane & 3) == 0) ldf[L_Y + w * 16 + (lane >> 2)] = ysum;
    u32x4 bfr[4];
    if (w < 5) {
      const unsigned short* wts = reinterpret_cast<const unsigned short*>(lds + L_WT) + (r * 8) * CB + w * 16 + c;
#pragma unroll
      for (int kk = 0; kk < 4; ++kk)
#pragma unroll
        for (int j = 0; j < 4; ++j)
          bfr[kk][j] = (unsigned)wts[(kk * 32 + 2 * j) * CB] | ((unsigned)wts[(kk * 32 + 2 * j + 1) * CB] << 16);
    }
    SB();
    bool ok = (g0[0] == tag) & (g0[2] == tag) & (g1[0] == tag) & (g1[2] == tag) & (gm[0] == tag) & (gm[2] == tag);
#pragma unroll 1
    for (unsigned spins = 0; spins < SPIN_CAP; ++spins) {
      const unsigned long long miss = __ballot(!ok);
      if (miss == 0) break;
      if (__popcll(miss) > BO_T1) __builtin_amdgcn_s_sleep(BO_S);
      if (!ok) {
        g0 = __builtin_amdgcn_raw_buffer_load_b128(rs, vo, ro, AUX_SC1);
        g1 = __builtin_amdgcn_raw_buffer_load_b128(rs, vo + 16, ro, AUX_SC1);
        gm = __builtin_amdgcn_raw_buffer_load_b128(rs, r * 2 * OREC + 1024, ro, AUX_SC1);
        ok = (g0[0] == tag) & (g0[2] == tag) & (g1[0] == tag) & (g1[2] == tag) & (gm[0] == tag) & (gm[2] == tag);
      }
      asm volatile("" ::: "memory");
    }
    STAMP(10);
    const float Ms = __uint_as_float(gm[1]);
    const float wgt = ex2(Ms - xrow_max(Ms));
    const float Ls = xrow_sum(wgt * __uint_as_float(gm[3]));
    const float t0 = swap32_add(wgt * __uint_as_float(g0[1]), wgt * __uint_as_float(g1[1]));
    const float t1 = swap32_add(wgt * __uint_as_float(g0[3]), wgt * __uint_as_float(g1[3]));
    const float ov = swap16_add(t0, t1) / Ls;
    reinterpret_cast<unsigned short*>(lds + L_O)[(w >> 1) * DH + (w & 1) * 64 + c * 4 + r] = (unsigned short)(cvtpk(ov, 0.f) & 0xffffu);
    LDS_BARRIER();
    if (w < 5) {
      f32x4 acc0 = {0.f, 0.f, 0.f, 0.f}, acc1 = {0.f, 0.f, 0.f, 0.f};
      u32x4 afr[4];
#pragma unroll
      for (int kk = 0; kk < 4; ++kk) afr[kk] = *reinterpret_cast<const u32x4*>(lds + L_O + (c & 3) * (DH / 2) + kk * 16 + r * 4);
      acc0 = __builtin_amdgcn_mfma_f32_16x16x32_bf16(__builtin_bit_cast(bf16x8, afr[0]), __builtin_bit_cast(bf16x8, bfr[0]), acc0, 0, 0, 0);
      acc1 = __builtin_amdgcn_mfma_f32_16x16x32_bf16(__builtin_bit_cast(bf16x8, afr[1]), __builtin_bit_cast(bf16x8, bfr[1]), acc1, 0, 0, 0);
      acc0 = __builtin_amdgcn_mfma_f32_16x16x32_bf16(__builtin_bit_cast(bf16x8, afr[2]), __builtin_bit_cast(bf16x8, bfr[2]), acc0, 0, 0, 0);
      acc1 = __builtin_amdgcn_mfma_f32_16x16x32_bf16(__builtin_bit_cast(bf16x8, afr[3]), __builtin_bit_cast(bf16x8, bfr[3]), acc1, 0, 0, 0);
      const int so = (lane < 16 && w * 16 + lane < CB) ? lane * HQ * 8 : 0x7fffffff;
#pragma unroll
      for (int i = 0; i < 4; ++i) {
        const u32x2 gs = {tag, __float_as_uint(acc0[i] + acc1[i])};
        __builtin_amdgcn_raw_buffer_store_b64(gs, rs, so, WS_SG + ((i * DM + cbk * CB + w * 16) * HQ + h1) * 8, AUX_SC1);
      }
      STAMP(11);
    } else {
      const int j = (w - 5) * 4 + r;
      const bool val = j < NOUT;
      const int so = WS_SG + ((b * DM + col0 + (w - 5) * 4) * HQ) * 8;
      const int vo2 = val ? lo16 : 0x7fffffff;
      u32x4 gs = __builtin_amdgcn_raw_buffer_load_b128(rs, vo2, so, AUX_SC1);
      SB();
      const int jj = val ? j : 0;
      float y = ldf[L_FLAG + 2];
#pragma unroll
      for (int ww = 0; ww < NWAVE; ++ww) y += ldf[L_Y + ww * 16 + jj];
      const float g = y / (1.0f + __expf(-y));
      const float xo = ldf[L_X + col0 + jj];
      SB();
      bool ok2 = !val | ((gs[0] == tag) & (gs[2] == tag));
#pragma unroll 1
    for (unsigned spins = 0; spins < SPIN_CAP; ++spins) {
        const unsigned long long miss = __ballot(!ok2);
        if (miss == 0) break;
        if (__popcll(miss) > BO_T2) __builtin_amdgcn_s_sleep(BO_S);
        if (!ok2) {
          gs = __builtin_amdgcn_raw_buffer_load_b128(rs, vo2, so, AUX_SC1);
          ok2 = (gs[0] == tag) & (gs[2] == tag);
        }
        asm volatile("" ::: "memory");
      }
      const float s = row_sum(__uint_as_float(gs[1]) + __uint_as_float(gs[3]));
      if (val && c == 0) out[b * DM + col0 + j] = (unsigned short)(cvtpk(fmaf(s, g, xo), 0.f) & 0xffffu);
      if (bid == 0 && w == 5 && lane == 0) __hip_atomic_store(reinterpret_cast<unsigned*>(ws + WS_EPOCH), tag, RLX_AGENT);
      STAMP(12);
    }
  }
}

}

extern "C" void kernel_launch(void* const* d_in, const int* in_sizes, int n_in, void* d_out, int out_size, void* d_ws, size_t ws_size,
                              hipStream_t stream) {
  (void)in_sizes; (void)n_in; (void)out_size; (void)ws_size;
  fused_decode_kernel<<<NWG, NTHR, 0, stream>>>(
      (const unsigned short*)d_in[0], (const unsigned short*)d_in[1], (const unsigned short*)d_in[2], (const unsigned short*)d_in[3],
      (const unsigned short*)d_in[4], (const unsigned short*)d_in[6], (const unsigned short*)d_in[7], (const unsigned short*)d_in[8],
      (const unsigned short*)d_in[9], (unsigned short*)d_out, (unsigned char*)d_ws);
}
```

```cpp
#include <hip/hip_runtime.h>
#include <hip/hip_bf16.h>
#include <stdint.h>

#define FAIR 0
#define DEPTH 2
#define BO_T1 16
#define BO_T2 8
#define BO_S 16
#define AUX_K 18
#define AUX_V 18
#define MAPV 3
#define ROTV 0

namespace {

constexpr int NB = 4, DM = 576, HQ = 32, HKV = 16, SK = 8192, DH = 128, FF = 4608;
constexpr int NWG = 256, NTHR = 512, NWAVE = 8;
constexpr int NSPLIT = 4;
constexpr int KEYS_WG = SK / NSPLIT;
constexpr int PAGE = 32;
constexpr int NPG = KEYS_WG / PAGE / NWAVE;
constexpr int QN = HQ * DH;
constexpr float LOG2E = 1.4426950408889634f;
constexpr float SC2 = 0.08838834764831845f * LOG2E;
constexpr float EPS = 1e-5f;
constexpr int CB = 72;
constexpr int NOUT = 9;

constexpr int WS_EPOCH = 0;
constexpr int WS_QG = 4096;
constexpr int WS_OG = WS_QG + NB * 4 * QN * 8;
constexpr int OREC = 1152;
constexpr int WS_SG = WS_OG + NWG * 2 * OREC;
constexpr int SROW = 640, SREC = NB * SROW;
constexpr int WS_BYTES = WS_SG + HQ * 8 * SREC;
constexpr unsigned TAG_STEP = 0x9E3779B1u;
constexpr unsigned SPIN_CAP = 1u << 20;

constexpr int L_XN = 0;
constexpr int L_QP = L_XN + DM * 4;
constexpr int L_QB = L_QP + NWAVE * 256;
constexpr int L_FLAG = L_QB + 128;
constexpr int L_H = L_FLAG + 4;
constexpr int L_X = L_H + DM;
constexpr int L_MW = L_X + DM;
constexpr int MW_W = 3 * 288;
constexpr int L_WT = L_MW + NWAVE * MW_W;
constexpr int L_O = L_WT + DH * CB / 2 + 8;
constexpr int L_Y = L_O + NB * DH / 2;
constexpr int L_WORDS = L_Y + NWAVE * 16;
static_assert(L_WORDS * 4 <= 163840, "LDS");
static_assert((L_QP % 4 | L_QB % 4 | L_MW % 4 | L_WT % 4 | L_O % 4 | L_FLAG % 2) == 0, "align");

typedef unsigned u32x4 __attribute__((ext_vector_type(4)));
typedef unsigned u32x2 __attribute__((ext_vector_type(2)));
typedef float f32x4 __attribute__((ext_vector_type(4)));
typedef __bf16 bf16x2 __attribute__((ext_vector_type(2)));
typedef __bf16 bf16x8 __attribute__((ext_vector_type(8)));
#define RLX_AGENT __ATOMIC_RELAXED, __HIP_MEMORY_SCOPE_AGENT
constexpr int AUX_SC1 = 16, AUX_NT = 2;

__device__ __forceinline__ float dot2(unsigned a, unsigned b, float acc) {
  return __builtin_amdgcn_fdot2_f32_bf16(__builtin_bit_cast(bf16x2, a), __builtin_bit_cast(bf16x2, b), acc, false);
}
__device__ __forceinline__ unsigned cvtpk(float lo, float hi) {
  bf16x2 r = {(__bf16)lo, (__bf16)hi};
  return __builtin_bit_cast(unsigned, r);
}
__device__ __forceinline__ float bf_lo(unsigned v) { return __builtin_bit_cast(float, v << 16); }
__device__ __forceinline__ float bf_hi(unsigned v) { return __builtin_bit_cast(float, v & 0xffff0000u); }
__device__ __forceinline__ float bf2f(unsigned short v) { return __builtin_bit_cast(float, (unsigned)v << 16); }
template <int CTRL> __device__ __forceinline__ float dpp(float x) {
  return __builtin_bit_cast(float, __builtin_amdgcn_mov_dpp(__builtin_bit_cast(int, x), CTRL, 0xf, 0xf, true));
}
constexpr int XOR1 = 0xB1, XOR2 = 0x4E, XOR7 = 0x141, ROR8 = 0x128, ROR4 = 0x124, BCAST = 0x150;
constexpr int QP0 = 0x00, QP1 = 0x55;
__device__ __forceinline__ float ex2(float x) { return __builtin_amdgcn_exp2f(x); }
__device__ __forceinline__ float rcp(float x) { return __builtin_amdgcn_rcpf(x); }

__device__ __forceinline__ float swap32_add(float a, float b) {
  auto s = __builtin_amdgcn_permlane32_swap(__float_as_uint(a), __float_as_uint(b), false, false);
  return __uint_as_float(s[0]) + __uint_as_float(s[1]);
}
__device__ __forceinline__ float swap16_add(float a, float b) {
  auto s = __builtin_amdgcn_permlane16_swap(__float_as_uint(a), __float_as_uint(b), false, false);
  return __uint_as_float(s[0]) + __uint_as_float(s[1]);
}
__device__ __forceinline__ float xrow_max(float x) {
  auto s = __builtin_amdgcn_permlane16_swap(__float_as_uint(x), __float_as_uint(x), false, false);
  x = fmaxf(__uint_as_float(s[0]), __uint_as_float(s[1]));
  auto t = __builtin_amdgcn_permlane32_swap(__float_as_uint(x), __float_as_uint(x), false, false);
  return fmaxf(__uint_as_float(t[0]), __uint_as_float(t[1]));
}
__device__ __forceinline__ float xrow_sum(float x) {
  x = swap16_add(x, x);
  return swap32_add(x, x);
}
__device__ __forceinline__ float row_sum(float x) {
  x += dpp<XOR1>(x);
  x += dpp<XOR2>(x);
  x += dpp<XOR7>(x);
  x += dpp<ROR8>(x);
  return x;
}
__device__ __forceinline__ float wave_sum(float x) { return xrow_sum(row_sum(x)); }
__device__ __forceinline__ float wave_treduce16(float (&v)[16], int lane) {
#pragma unroll
  for (int j = 0; j < 8; ++j) v[j] = swap32_add(v[j], v[j + 8]);
#pragma unroll
  for (int j = 0; j < 4; ++j) v[j] = swap16_add(v[j], v[j + 4]);
#pragma unroll
  for (int j = 0; j < 2; ++j) {
    const float a = v[j] + dpp<ROR8>(v[j]), b = v[j + 2] + dpp<ROR8>(v[j + 2]);
    v[j] = (lane & 8) ? b : a;
  }
  {
    const float a = v[0] + dpp<XOR7>(v[0]), b = v[1] + dpp<XOR7>(v[1]);
    v[0] = (lane & 4) ? b : a;
  }
  float r = v[0];
  r += dpp<XOR2>(r);
  r += dpp<XOR1>(r);
  return r;
}

#define SB() __builtin_amdgcn_sched_barrier(0)
#define STAMP(n)
#define PIN8(a) asm volatile("" : "+v"(a[0]), "+v"(a[1]), "+v"(a[2]), "+v"(a[3]), "+v"(a[4]), "+v"(a[5]), "+v"(a[6]), "+v"(a[7]) : : "memory")
#define LDS_BARRIER() do { asm volatile("s_waitcnt lgkmcnt(0)" ::: "memory"); __builtin_amdgcn_s_barrier(); asm volatile("" ::: "memory"); } while (0)

__global__ void __launch_bounds__(NTHR, 2)
fused_decode_kernel(const unsigned short* __restrict__ x, const unsigned short* __restrict__ kc, const unsigned short* __restrict__ vc,
                    const unsigned short* __restrict__ wq, const unsigned short* __restrict__ wo, const unsigned short* __restrict__ w1,
                    const unsigned short* __restrict__ b1, const unsigned short* __restrict__ w2, const unsigned short* __restrict__ b2,
                    unsigned short* __restrict__ out, unsigned char* ws) {
  __shared__ __attribute__((aligned(16))) unsigned lds[L_WORDS];
  float* ldf = reinterpret_cast<float*>(lds);
  const int tid = threadIdx.x, lane = tid & 63, w = __builtin_amdgcn_readfirstlane(tid >> 6);
  const int r = lane >> 4, c = lane & 15;
  const int bid = blockIdx.x, xcd = bid & 7, ii = bid >> 3;
  const int kvh = ii & 15, b = (xcd >> 1) & 3, sp = ((xcd & 1) << 1) | (ii >> 4);
  const int rot = 0;
  const int prod = b * 64 + kvh * 4 + sp;
  const int col0 = (prod & 63) * NOUT;
  const int h1 = 4 * xcd + (ii >> 3), cbk = ii & 7;

  STAMP(0);
  const unsigned tag = *reinterpret_cast<const __attribute__((address_space(4))) unsigned*>(reinterpret_cast<uintptr_t>(ws + WS_EPOCH)) + TAG_STEP;
  const int lnb = w < 4 ? w : b;
  const __amdgpu_buffer_rsrc_t rx = __builtin_amdgcn_make_buffer_rsrc((void*)x, 0, NB * DM * 2, 0x00020000);
  const __amdgpu_buffer_rsrc_t rb2 = __builtin_amdgcn_make_buffer_rsrc((void*)b2, 0, 2, 0x00020000);
  unsigned short xr[9];
#pragma unroll
  for (int j = 0; j < 9; ++j) xr[j] = __builtin_amdgcn_raw_buffer_load_b16(rx, lane * 2 + j * 128, w <= 4 ? lnb * DM * 2 : 0x10000, 0);
  const unsigned short b2r = __builtin_amdgcn_raw_buffer_load_b16(rb2, 0, w == 4 ? 0 : 0x10000, 0);
  const size_t kvbase = ((size_t)(b * HKV + kvh) * SK + (size_t)sp * KEYS_WG) * DH;
  const __amdgpu_buffer_rsrc_t rk = __builtin_amdgcn_make_buffer_rsrc((void*)(kc + kvbase), 0, KEYS_WG * DH * 2, 0x00020000);
  const __amdgpu_buffer_rsrc_t rv = __builtin_amdgcn_make_buffer_rsrc((void*)(vc + kvbase), 0, KEYS_WG * DH * 2, 0x00020000);
  const __amdgpu_buffer_rsrc_t rwq = __builtin_amdgcn_make_buffer_rsrc((void*)wq, 0, DM * QN * 2, 0x00020000);
  const __amdgpu_buffer_rsrc_t rwo = __builtin_amdgcn_make_buffer_rsrc((void*)wo, 0, DM * QN * 2, 0x00020000);
  const __amdgpu_buffer_rsrc_t rs = __builtin_amdgcn_make_buffer_rsrc((void*)ws, 0, WS_BYTES, 0x00020000);
  constexpr int PBYTES = PAGE * DH * 2;
  constexpr int PSTR = NWAVE * PBYTES;
  const int lo16 = lane * 16;
  const int wpo = w * PBYTES + rot * PSTR;
#define LDK(i, t) __builtin_amdgcn_raw_buffer_load_b128(rk, lo16 + ((t) & 3) * 1024, wpo + (i) * PSTR + ((t) >> 2) * 4096, AUX_K)
#define LDV(i, t) __builtin_amdgcn_raw_buffer_load_b128(rv, lo16 + ((t) & 3) * 1024, wpo + (i) * PSTR + ((t) >> 2) * 4096, AUX_V)
  u32x4 kb0[8], vb0[8], kb1[8], vb1[8];
  const int cb = bid & 63, ks = bid >> 6;
  const int qrow = ks * 144 + w * 18 + (lane >> 3);
  u32x4 wqr[3];
  {
    const int wqv = (lane >> 3) * QN * 2 + (lane & 7) * 16;
    const int wqs = ((ks * 144 + w * 18) * QN + cb * 64) * 2;
    wqr[0] = __builtin_amdgcn_raw_buffer_load_b128(rwq, wqv, wqs, AUX_NT);
    wqr[1] = __builtin_amdgcn_raw_buffer_load_b128(rwq, wqv, wqs + 8 * QN * 2, AUX_NT);
    wqr[2] = __builtin_amdgcn_raw_buffer_load_b128(rwq, lane < 16 ? wqv : 0x7fffffff, wqs + 16 * QN * 2, AUX_NT);
  }
  SB();
  asm volatile("" ::: "memory");
  __builtin_amdgcn_s_barrier();
  asm volatile("" ::: "memory");
  SB();
#pragma unroll
  for (int t = 0; t < 8; ++t) { kb0[t] = LDK(0, t); SB(); }
  asm volatile("" :: "v"(xr[0]), "v"(xr[1]), "v"(xr[2]), "v"(xr[3]), "v"(xr[4]), "v"(xr[5]), "v"(xr[6]), "v"(xr[7]), "v"(xr[8]), "v"(b2r));
  SB();

  if (w <= 4) {
    float xv[9];
    float s = 0.f;
#pragma unroll
    for (int j = 0; j < 9; ++j) { xv[j] = bf2f(xr[j]); s += xv[j]; }
    if (w == 4) {
#pragma unroll
      for (int j = 0; j < 9; ++j) ldf[L_X + j * 64 + lane] = xv[j];
      ldf[L_FLAG + 2] = bf2f(b2r);
    }
    const float mean = wave_sum(s) * (1.0f / DM);
    float v = 0.f;
#pragma unroll
    for (int j = 0; j < 9; ++j) { xv[j] -= mean; v = fmaf(xv[j], xv[j], v); }
    const float rstd = 1.0f / sqrtf(wave_sum(v) * (1.0f / DM) + EPS);
    if (w < 4) {
#pragma unroll
      for (int j = 0; j < 9; ++j) lds[L_XN + (j * 64 + lane) * 4 + w] = cvtpk(xv[j] * rstd, 0.f) & 0xffffu;
    } else {
      float s2 = 0.f;
#pragma unroll
      for (int j = 0; j < 9; ++j) { xv[j] = bf_lo(cvtpk(xv[j] * rstd, 0.f)); s2 += xv[j]; }
      const float mean2 = wave_sum(s2) * (1.0f / DM);
      float v2 = 0.f;
#pragma unroll
      for (int j = 0; j < 9; ++j) { xv[j] -= mean2; v2 = fmaf(xv[j], xv[j], v2); }
      const float rstd2 = 1.0f / sqrtf(wave_sum(v2) * (1.0f / DM) + EPS);
#pragma unroll
      for (int j = 0; j < 9; ++j) ldf[L_H + j * 64 + lane] = xv[j] * rstd2;
    }
  } else if (w == 5) {
    if (lane < 2) lds[L_FLAG + lane] = 0u;
  }
  LDS_BARRIER();
  STAMP(1);
  {
    float qa[32];
#pragma unroll
    for (int k = 0; k < 32; ++k) qa[k] = 0.f;
#pragma unroll
    for (int j = 0; j < 3; ++j) {
      const int row = (j < 2) ? qrow + 8 * j : min(qrow + 16, DM - 1);
      const u32x4 xq = *reinterpret_cast<const u32x4*>(lds + L_XN + row * 4);
#pragma unroll
      for (int bb = 0; bb < 4; ++bb) {
        const unsigned xl = xq[bb], xh = xq[bb] << 16;
#pragma unroll
        for (int k = 0; k < 4; ++k) {
          qa[bb * 8 + 2 * k] = dot2(wqr[j][k], xl, qa[bb * 8 + 2 * k]);
          qa[bb * 8 + 2 * k + 1] = dot2(wqr[j][k], xh, qa[bb * 8 + 2 * k + 1]);
        }
      }
    }
#pragma unroll
    for (int k = 0; k < 16; ++k) qa[k] = swap32_add(qa[k], qa[k + 16]);
#pragma unroll
    for (int k = 0; k < 8; ++k) qa[k] = swap16_add(qa[k], qa[k + 8]);
    f32x4 qs;
#pragma unroll
    for (int k = 0; k < 4; ++k) {
      const float t1 = qa[k] + dpp<ROR8>(qa[k]), t2 = qa[k + 4] + dpp<ROR8>(qa[k + 4]);
      qs[k] = (lane & 8) ? t2 : t1;
    }
    *reinterpret_cast<f32x4*>(ldf + L_QP + w * 256 + (lane >> 4) * 64 + (lane & 7) * 8 + ((lane >> 3) & 1) * 4) = qs;
  }
  LDS_BARRIER();
  STAMP(2);
  if (w < 2) {
    float q0 = 0.f, q1 = 0.f;
#pragma unroll
    for (int ww = 0; ww < NWAVE; ++ww) {
      const float2 t = *reinterpret_cast<const float2*>(ldf + L_QP + ww * 256 + tid * 2);
      q0 += t.x; q1 += t.y;
    }
    const u32x4 gq = {tag, __float_as_uint(q0), tag, __float_as_uint(q1)};
    __builtin_amdgcn_raw_buffer_store_b128(gq, rs, WS_QG + (((tid >> 5) * 4 + ks) * QN + cb * 64 + (tid & 31) * 2) * 8, 0, AUX_SC1);
    const int qo = WS_QG + ((b * 4) * QN + kvh * 256 + tid * 2) * 8;
    u32x4 gv[4];
    bool ok = false;
#pragma unroll 1
    for (unsigned spins = 0; spins < SPIN_CAP; ++spins) {
      if (!ok) {
#pragma unroll
        for (int s = 0; s < 4; ++s) gv[s] = __builtin_amdgcn_raw_buffer_load_b128(rs, qo, s * QN * 8, AUX_SC1);
        ok = true;
#pragma unroll
        for (int s = 0; s < 4; ++s) ok &= (gv[s][0] == tag) & (gv[s][2] == tag);
      }
      if (__all(ok)) break;
      asm volatile("" ::: "memory");
    }
    q0 = (__uint_as_float(gv[0][1]) + __uint_as_float(gv[1][1])) + (__uint_as_float(gv[2][1]) + __uint_as_float(gv[3][1]));
    q1 = (__uint_as_float(gv[0][3]) + __uint_as_float(gv[1][3])) + (__uint_as_float(gv[2][3]) + __uint_as_float(gv[3][3]));
    lds[L_QB + tid] = cvtpk(q0, q1);
    asm volatile("" ::: "memory");
    if (lane == 0) __hip_atomic_store(lds + L_FLAG + w, 1u, __ATOMIC_RELAXED, __HIP_MEMORY_SCOPE_WORKGROUP);
    asm volatile("" ::: "memory");
  }
  auto q_ready = [&]() {
    const unsigned long long f = __hip_atomic_load(reinterpret_cast<unsigned long long*>(lds + L_FLAG), __ATOMIC_RELAXED, __HIP_MEMORY_SCOPE_WORKGROUP);
    return __builtin_amdgcn_readfirstlane((unsigned)f & (unsigned)(f >> 32)) != 0;
  };
  bool ready = false;
#define CHUNK(dst, LD, pg, half) do { SB(); _Pragma("unroll") for (int t = 4 * (half); t < 4 * (half) + 4; ++t) dst[t] = LD(pg, t); SB(); \
    if (!ready) { asm volatile("s_waitcnt vmcnt(4)" ::: "memory"); ready = q_ready(); } SB(); } while (0)
  CHUNK(vb0, LDV, 0, 0);
  CHUNK(vb0, LDV, 0, 1);
  CHUNK(kb1, LDK, 1, 0);
  CHUNK(kb1, LDK, 1, 1);
  CHUNK(vb1, LDV, 1, 0);
#pragma unroll
  for (int t = 4; t < 8; ++t) vb1[t] = LDV(1, t);
  SB();
  u32x4 mw[4], wt[3];
#pragma unroll
  for (int j = 0; j < 3; ++j) {
    const unsigned short* src = j == 0 ? w1 : (j == 1 ? b1 : w2);
    mw[j] = reinterpret_cast<const u32x4*>(src)[72 * w + lane];
  }
  {
    const int a = lane >> 3;
    const unsigned short* src = a == 0 ? w1 : (a == 1 ? b1 : w2);
    mw[3] = reinterpret_cast<const u32x4*>(src)[72 * w + 64 + (lane & 7)];
  }
#pragma unroll
  for (int j = 0; j < 3; ++j) {
    const int ch = tid + NTHR * j;
    const int row = (ch * 7282) >> 16;
    const int vo = row * (DM * 2) + (ch - row * 9) * 16;
    wt[j] = __builtin_amdgcn_raw_buffer_load_b128(rwo, (j < 2 || tid < 128) ? vo : 0x7fffffff, (h1 * DH * DM + cbk * CB) * 2, AUX_NT);
  }
  SB();
  while (!ready) { __builtin_amdgcn_s_sleep(1); ready = q_ready(); }
  asm volatile("" ::: "memory");
  const u32x4 qh0 = *reinterpret_cast<const u32x4*>(lds + L_QB + c * 4);
  const u32x4 qh1 = *reinterpret_cast<const u32x4*>(lds + L_QB + 64 + c * 4);
  float hv[NOUT];
  float ya[16];
#pragma unroll
  for (int j = 0; j < 16; ++j) ya[j] = 0.f;
#pragma unroll
  for (int j = 0; j < NOUT; ++j) hv[j] = __builtin_bit_cast(float, __builtin_amdgcn_readfirstlane(lds[L_H + col0 + j]));
  const unsigned short* mwl = reinterpret_cast<const unsigned short*>(lds + L_MW + w * MW_W);

  auto mlp_slice = [&](int k) {
    const int f = k * 64 + lane;
    const float w1f = bf2f(mwl[f]), b1f = bf2f(mwl[576 + f]), w2f = bf2f(mwl[2 * 576 + f]);
    constexpr float C1 = -2.0f * 0.7978845608028654f * LOG2E, C2 = C1 * 0.044715f;
#pragma unroll
    for (int j = 0; j < NOUT; ++j) {
      const float uu = fmaf(hv[j], w1f, b1f);
      const float e = ex2(uu * fmaf(uu * uu, C2, C1));
      ya[j] = fmaf(uu * w2f, rcp(1.0f + e), ya[j]);
    }
  };

  float m = -INFINITY, l = 0.f;
  float a0[8], a1[8];
#pragma unroll
  for (int k = 0; k < 8; ++k) { a0[k] = 0.f; a1[k] = 0.f; }

#define ISSUE(N, so, first) do { if (!(first) || (N) >= 8) { \
      _Pragma("unroll") for (int t = 0; t < 4; ++t) { \
        const int o_ = ((N) < 8 ? (so) : so_n) + (((N) >> 2) & 1) * PSTR + ((N) & 1) * 4096; \
        if (((N) & 6) == 0) kb0[4 * ((N) & 1) + t] = __builtin_amdgcn_raw_buffer_load_b128(rk, lo16 + t * 1024, o_, AUX_K); \
        if (((N) & 6) == 2) vb0[4 * ((N) & 1) + t] = __builtin_amdgcn_raw_buffer_load_b128(rv, lo16 + t * 1024, o_, AUX_V); \
        if (((N) & 6) == 4) kb1[4 * ((N) & 1) + t] = __builtin_amdgcn_raw_buffer_load_b128(rk, lo16 + t * 1024, o_, AUX_K); \
        if (((N) & 6) == 6) vb1[4 * ((N) & 1) + t] = __builtin_amdgcn_raw_buffer_load_b128(rv, lo16 + t * 1024, o_, AUX_V); \
        SB(); } } } while (0)
#define QK_HALF(kb, hf) _Pragma("unroll") for (int t = 4 * (hf); t < 4 * (hf) + 4; ++t) { \
      float d0 = 0.f, d1 = 0.f; \
      _Pragma("unroll") for (int k = 0; k < 4; ++k) { d0 = dot2(kb[t][k], qh0[k], d0); d1 = dot2(kb[t][k], qh1[k], d1); } \
      xs[2 * t] = d0; xs[2 * t + 1] = d1; }
#define TR_STEP(HALF, CTRL, BIT) _Pragma("unroll") for (int h = 0; h < (HALF); ++h) { \
      const float t1_ = xs[h] + dpp<CTRL>(xs[h]), t2_ = xs[h + (HALF)] + dpp<CTRL>(xs[h + (HALF)]); \
      xs[h] = (c & (BIT)) ? t2_ : t1_; }
#define PV_STEP(vb, t) { const float p0 = dpp<BCAST + 2 * (t)>(p), p1 = dpp<BCAST + 2 * (t) + 1>(p); \
      _Pragma("unroll") for (int k = 0; k < 4; ++k) { const float lo = bf_lo(vb[t][k]), hi = bf_hi(vb[t][k]); \
        a0[2 * k] = fmaf(p0, lo, a0[2 * k]); a0[2 * k + 1] = fmaf(p0, hi, a0[2 * k + 1]); \
        a1[2 * k] = fmaf(p1, lo, a1[2 * k]); a1[2 * k + 1] = fmaf(p1, hi, a1[2 * k + 1]); } }
#define PAGE_STEP(kb, vb, S, so, first) do { \
    float xs[16]; \
    QK_HALF(kb, 0) \
    PIN8(xs); SB(); \
    ISSUE((S) + DEPTH, so, first); \
    QK_HALF(kb, 1) \
    PIN8((xs + 8)); SB(); \
    ISSUE((S) + 1 + DEPTH, so, first); \
    TR_STEP(8, ROR8, 8) TR_STEP(4, XOR7, 4) TR_STEP(2, XOR2, 2) TR_STEP(1, XOR1, 1) \
    const float sc = xs[0] * SC2; \
    float pm = fmaxf(sc, dpp<XOR2>(sc)); \
    pm = fmaxf(pm, dpp<ROR4>(pm)); \
    pm = fmaxf(pm, dpp<ROR8>(pm)); \
    const float mn = fmaxf(m, pm); \
    const float alpha = ex2(m - mn), p = ex2(sc - mn); \
    l = fmaf(l, alpha, p); \
    m = mn; \
    const float al0 = dpp<QP0>(alpha), al1 = dpp<QP1>(alpha); \
    _Pragma("unroll") for (int k = 0; k < 8; ++k) { a0[k] *= al0; a1[k] *= al1; } \
    PV_STEP(vb, 0) PV_STEP(vb, 1) PV_STEP(vb, 2) PV_STEP(vb, 3) \
    PIN8(a0); PIN8(a1); SB(); \
    ISSUE((S) + 2 + DEPTH, so, first); \
    PV_STEP(vb, 4) PV_STEP(vb, 5) PV_STEP(vb, 6) PV_STEP(vb, 7) \
    PIN8(a0); PIN8(a1); SB(); \
    ISSUE((S) + 3 + DEPTH, so, first); \
  } while (0)

  STAMP(3);
  int so_n = w * PBYTES + ((rot + 2) & 7) * PSTR;
  PAGE_STEP(kb0, vb0, 0, wpo, true);
  STAMP(4);
  PAGE_STEP(kb1, vb1, 4, wpo, true);
  {
    unsigned* mwd = lds + L_MW + w * MW_W;
#pragma unroll
    for (int j = 0; j < 3; ++j) *reinterpret_cast<u32x4*>(mwd + j * 288 + lane * 4) = mw[j];
    if (lane < 24) *reinterpret_cast<u32x4*>(mwd + (lane >> 3) * 288 + 256 + (lane & 7) * 4) = mw[3];
#pragma unroll
    for (int j = 0; j < 2; ++j) *reinterpret_cast<u32x4*>(lds + L_WT + (tid + NTHR * j) * 4) = wt[j];
    if (tid < 128) *reinterpret_cast<u32x4*>(lds + L_WT + (tid + NTHR * 2) * 4) = wt[2];
    asm volatile("" ::: "memory");
    mlp_slice(0);
    mlp_slice(1);
    mlp_slice(2);
  }
  SB();
  STAMP(5);
#pragma unroll 1
  for (int i = 2; i < NPG; i += 2) {
    const int so = so_n;
    so_n = i + 2 < NPG ? w * PBYTES + ((rot + i + 2) & 7) * PSTR : 0x40000000;
    mlp_slice(i + 1);
    SB();
    PAGE_STEP(kb0, vb0, 0, so, false);
    mlp_slice(i + 2);
    SB();
    PAGE_STEP(kb1, vb1, 4, so, false);
  }
  STAMP(7);

  {
    const float mw_ = xrow_max(m);
    const float sc_ = ex2(m - mw_);
    float lr = l * sc_;
    lr += dpp<XOR2>(lr);
    lr += dpp<ROR4>(lr);
    lr += dpp<ROR8>(lr);
    lr = xrow_sum(lr);
    const float s0 = dpp<QP0>(sc_), s1 = dpp<QP1>(sc_);
    float t8[8];
#pragma unroll
    for (int k = 0; k < 8; ++k) t8[k] = swap32_add(a0[k] * s0, a1[k] * s1);
    f32x4 t4;
#pragma unroll
    for (int k = 0; k < 4; ++k) t4[k] = swap16_add(t8[k], t8[k + 4]);
    float* rec = ldf + L_MW + w * MW_W;
    *reinterpret_cast<f32x4*>(rec + (lane >> 5) * 128 + c * 8 + (r & 1) * 4) = t4;
    if (lane < 2) { rec[256 + lane * 2] = mw_; rec[257 + lane * 2] = lr; }
  }
  LDS_BARRIER();
  STAMP(8);
  if (w < 2) {
    float mm[NWAVE], ll[NWAVE];
    float M = -INFINITY;
#pragma unroll
    for (int ww = 0; ww < NWAVE; ++ww) {
      const float2 t = *reinterpret_cast<const float2*>(ldf + L_MW + ww * MW_W + 256 + w * 2);
      mm[ww] = t.x; ll[ww] = t.y; M = fmaxf(M, t.x);
    }
    float O0 = 0.f, O1 = 0.f, L = 0.f;
#pragma unroll
    for (int ww = 0; ww < NWAVE; ++ww) {
      const float wgt = ex2(mm[ww] - M);
      L = fmaf(wgt, ll[ww], L);
      const float2 t = *reinterpret_cast<const float2*>(ldf + L_MW + ww * MW_W + w * 128 + lane * 2);
      O0 = fmaf(wgt, t.x, O0); O1 = fmaf(wgt, t.y, O1);
    }
    const int ro = WS_OG + (prod * 2 + w) * OREC;
    const u32x4 go = {tag, __float_as_uint(O0), tag, __float_as_uint(O1)};
    __builtin_amdgcn_raw_buffer_store_b128(go, rs, lo16, ro, AUX_SC1);
    const u32x4 gm = {tag, __float_as_uint(M), tag, __float_as_uint(L)};
    __builtin_amdgcn_raw_buffer_store_b128(gm, rs, lane == 0 ? 1024 : 0x7fffffff, ro, AUX_SC1);
  }
  STAMP(9);
  {
    const int ro = WS_OG + (((w >> 1) * 64 + (h1 >> 1) * 4) * 2 + (h1 & 1)) * OREC;
    const int vo = r * 2 * OREC + ((w & 1) * 64 + c * 4) * 8;
    u32x4 g0, g1, gm;
    g0 = __builtin_amdgcn_raw_buffer_load_b128(rs, vo, ro, AUX_SC1);
    g1 = __builtin_amdgcn_raw_buffer_load_b128(rs, vo + 16, ro, AUX_SC1);
    gm = __builtin_amdgcn_raw_buffer_load_b128(rs, r * 2 * OREC + 1024, ro, AUX_SC1);
    SB();
    const float ysum = wave_treduce16(ya, lane);
    if ((lane & 3) == 0) ldf[L_Y + w * 16 + (lane >> 2)] = ysum;
    u32x4 bfr[4];
    if (w < 5) {
      const unsigned short* wts = reinterpret_cast<const unsigned short*>(lds + L_WT) + (r * 8) * CB + w * 16 + c;
#pragma unroll
      for (int kk = 0; kk < 4; ++kk)
#pragma unroll
        for (int j = 0; j < 4; ++j)
          bfr[kk][j] = (unsigned)wts[(kk * 32 + 2 * j) * CB] | ((unsigned)wts[(kk * 32 + 2 * j + 1) * CB] << 16);
    }
    SB();
    bool ok = (g0[0] == tag) & (g0[2] == tag) & (g1[0] == tag) & (g1[2] == tag) & (gm[0] == tag) & (gm[2] == tag);
#pragma unroll 1
    for (unsigned spins = 0; spins < SPIN_CAP; ++spins) {
      const unsigned long long miss = __ballot(!ok);
      if (miss == 0) break;
      if (__popcll(miss) > BO_T1) __builtin_amdgcn_s_sleep(BO_S);
      if (!ok) {
        g0 = __builtin_amdgcn_raw_buffer_load_b128(rs, vo, ro, AUX_SC1);
        g1 = __builtin_amdgcn_raw_buffer_load_b128(rs, vo + 16, ro, AUX_SC1);
        gm = __builtin_amdgcn_raw_buffer_load_b128(rs, r * 2 * OREC + 1024, ro, AUX_SC1);
        ok = (g0[0] == tag) & (g0[2] == tag) & (g1[0] == tag) & (g1[2] == tag) & (gm[0] == tag) & (gm[2] == tag);
      }
      asm volatile("" ::: "memory");
    }
    STAMP(10);
    const float Ms = __uint_as_float(gm[1]);
    const float wgt = ex2(Ms - xrow_max(Ms));
    const float Ls = xrow_sum(wgt * __uint_as_float(gm[3]));
    const float t0 = swap32_add(wgt * __uint_as_float(g0[1]), wgt * __uint_as_float(g1[1]));
    const float t1 = swap32_add(wgt * __uint_as_float(g0[3]), wgt * __uint_as_float(g1[3]));
    const float ov = swap16_add(t0, t1) * rcp(Ls);
    reinterpret_cast<unsigned short*>(lds + L_O)[(w >> 1) * DH + (w & 1) * 64 + c * 4 + r] = (unsigned short)(cvtpk(ov, 0.f) & 0xffffu);
    LDS_BARRIER();
    if (w < 5) {
      f32x4 acc0 = {0.f, 0.f, 0.f, 0.f}, acc1 = {0.f, 0.f, 0.f, 0.f};
      u32x4 afr[4];
#pragma unroll
      for (int kk = 0; kk < 4; ++kk) afr[kk] = *reinterpret_cast<const u32x4*>(lds + L_O + (c & 3) * (DH / 2) + kk * 16 + r * 4);
      acc0 = __builtin_amdgcn_mfma_f32_16x16x32_bf16(__builtin_bit_cast(bf16x8, bfr[0]), __builtin_bit_cast(bf16x8, afr[0]), acc0, 0, 0, 0);
      acc1 = __builtin_amdgcn_mfma_f32_16x16x32_bf16(__builtin_bit_cast(bf16x8, bfr[1]), __builtin_bit_cast(bf16x8, afr[1]), acc1, 0, 0, 0);
      acc0 = __builtin_amdgcn_mfma_f32_16x16x32_bf16(__builtin_bit_cast(bf16x8, bfr[2]), __builtin_bit_cast(bf16x8, afr[2]), acc0, 0, 0, 0);
      acc1 = __builtin_amdgcn_mfma_f32_16x16x32_bf16(__builtin_bit_cast(bf16x8, bfr[3]), __builtin_bit_cast(bf16x8, afr[3]), acc1, 0, 0, 0);
      const int so = (c < 4 && w * 16 + r * 4 < CB) ? c * SROW + r * 32 : 0x7fffffff;
      const int sb = WS_SG + (h1 * 8 + cbk) * SREC + w * 128;
      const u32x4 s0 = {tag, __float_as_uint(acc0[0] + acc1[0]), tag, __float_as_uint(acc0[1] + acc1[1])};
      const u32x4 s1 = {tag, __float_as_uint(acc0[2] + acc1[2]), tag, __float_as_uint(acc0[3] + acc1[3])};
      __builtin_amdgcn_raw_buffer_store_b128(s0, rs, so, sb, AUX_SC1);
      __builtin_amdgcn_raw_buffer_store_b128(s1, rs, so, sb + 16, AUX_SC1);
      STAMP(11);
    } else {
      const int odd = prod & 1, pr = 2 * (w - 5) + (r >> 1);
      const int j0 = 2 * pr - odd;
      const bool v0 = j0 >= 0 && j0 < NOUT, v1 = j0 + 1 < NOUT;
      const int so = WS_SG + ((prod & 63) >> 3) * SREC + b * SROW + ((prod & 7) * NOUT - odd) * 8;
      const int vo2 = pr < 5 ? ((r & 1) * 16 + c) * 8 * SREC + pr * 16 : 0x7fffffff;
      u32x4 gs = __builtin_amdgcn_raw_buffer_load_b128(rs, vo2, so, AUX_SC1);
      SB();
      const int jj = min(max(j0 + (c & 1), 0), NOUT - 1);
      float y = ldf[L_FLAG + 2];
#pragma unroll
      for (int ww = 0; ww < NWAVE; ++ww) y += ldf[L_Y + ww * 16 + jj];
      const float g = y / (1.0f + __expf(-y));
      const float xo = ldf[L_X + col0 + jj];
      SB();
      bool ok2 = (!v0 | (gs[0] == tag)) & (!v1 | (gs[2] == tag));
#pragma unroll 1
      for (unsigned spins = 0; spins < SPIN_CAP; ++spins) {
        if (__all(ok2)) break;
        if (!ok2) {
          gs = __builtin_amdgcn_raw_buffer_load_b128(rs, vo2, so, AUX_SC1);
          ok2 = (!v0 | (gs[0] == tag)) & (!v1 | (gs[2] == tag));
        }
        asm volatile("" ::: "memory");
      }
      const float e0 = __uint_as_float(gs[1]), e1 = __uint_as_float(gs[3]);
      const float u0 = e0 + dpp<XOR1>(e0), u1 = e1 + dpp<XOR1>(e1);
      float s = (c & 1) ? u1 : u0;
      s += dpp<XOR2>(s);
      s += dpp<ROR4>(s);
      s += dpp<ROR8>(s);
      s = swap16_add(s, s);
      if ((r & 1) == 0 && c < 2 && (c ? v1 : v0)) out[b * DM + col0 + j0 + c] = (unsigned short)(cvtpk(fmaf(s, g, xo), 0.f) & 0xffffu);
      if (bid == 0 && w == 5 && lane == 0) __hip_atomic_store(reinterpret_cast<unsigned*>(ws + WS_EPOCH), tag, RLX_AGENT);
      STAMP(12);
    }
  }
}

}

extern "C" void kernel_launch(void* const* d_in, const int* in_sizes, int n_in, void* d_out, int out_size, void* d_ws, size_t ws_size,
                              hipStream_t stream) {
  (void)in_sizes; (void)n_in; (void)out_size; (void)ws_size;
  fused_decode_kernel<<<NWG, NTHR, 0, stream>>>(
      (const unsigned short*)d_in[0], (const unsigned short*)d_in[1], (const unsigned short*)d_in[2], (const unsigned short*)d_in[3],
      (const unsigned short*)d_in[4], (const unsigned short*)d_in[6], (const unsigned short*)d_in[7], (const unsigned short*)d_in[8],
      (const unsigned short*)d_in[9], (unsigned short*)d_out, (unsigned char*)d_ws);
}
```

```cpp
#include <hip/hip_runtime.h>
#include <hip/hip_bf16.h>
#include <stdint.h>

namespace {

constexpr int NB = 4, DM = 576, HQ = 32, HKV = 16, SK = 8192, DH = 128, FF = 4608;
constexpr int NWG = 256, NTHR = 512, NWAVE = 8;
constexpr int NSPLIT = 4;
constexpr int KEYS_WG = SK / NSPLIT;
constexpr int PAGE = 32;
constexpr int NPG = KEYS_WG / PAGE / NWAVE;
constexpr int QN = HQ * DH;
constexpr float LOG2E = 1.4426950408889634f;
constexpr float SC2 = 0.08838834764831845f * LOG2E;
constexpr float EPS = 1e-5f;
constexpr int DEPTH = 2;
constexpr int CB = 72;
constexpr int NOUT = 9;

constexpr int WS_EPOCH = 0;
constexpr int WS_QG = 4096;
constexpr int WS_OG = WS_QG + NB * 4 * QN * 8;
constexpr int OREC = 1152;
constexpr int WS_SG = WS_OG + NWG * 2 * OREC;
constexpr int SROW = 640, SREC = NB * SROW;
constexpr int WS_BYTES = WS_SG + HQ * 8 * SREC;
constexpr unsigned TAG_STEP = 0x9E3779B1u;
constexpr unsigned SPIN_CAP = 1u << 20;

constexpr int L_XN = 0;
constexpr int L_QP = L_XN + DM * 4;
constexpr int L_QB = L_QP + NWAVE * 256;
constexpr int L_FLAG = L_QB + 128;
constexpr int L_H = L_FLAG + 4;
constexpr int L_X = L_H + DM;
constexpr int L_MW = L_X + DM;
constexpr int MW_W = 3 * 288;
constexpr int L_WT = L_MW + NWAVE * MW_W;
constexpr int L_O = L_WT + DH * CB / 2 + 8;
constexpr int L_Y = L_O + NB * DH / 2;
constexpr int L_WORDS = L_Y + NWAVE * 16;
static_assert(L_WORDS * 4 <= 163840, "LDS");
static_assert((L_QP % 4 | L_QB % 4 | L_MW % 4 | L_WT % 4 | L_O % 4 | L_FLAG % 2) == 0, "align");

typedef unsigned u32x4 __attribute__((ext_vector_type(4)));
typedef unsigned u32x2 __attribute__((ext_vector_type(2)));
typedef float f32x4 __attribute__((ext_vector_type(4)));
typedef __bf16 bf16x2 __attribute__((ext_vector_type(2)));
typedef __bf16 bf16x8 __attribute__((ext_vector_type(8)));
#define RLX_AGENT __ATOMIC_RELAXED, __HIP_MEMORY_SCOPE_AGENT
constexpr int AUX_SC1 = 16, AUX_NT = 2, AUX_KV = 18;

__device__ __forceinline__ float dot2(unsigned a, unsigned b, float acc) {
  return __builtin_amdgcn_fdot2_f32_bf16(__builtin_bit_cast(bf16x2, a), __builtin_bit_cast(bf16x2, b), acc, false);
}
__device__ __forceinline__ unsigned cvtpk(float lo, float hi) {
  bf16x2 r = {(__bf16)lo, (__bf16)hi};
  return __builtin_bit_cast(unsigned, r);
}
__device__ __forceinline__ float bf_lo(unsigned v) { return __builtin_bit_cast(float, v << 16); }
__device__ __forceinline__ float bf_hi(unsigned v) { return __builtin_bit_cast(float, v & 0xffff0000u); }
__device__ __forceinline__ float bf2f(unsigned short v) { return __builtin_bit_cast(float, (unsigned)v << 16); }
template <int CTRL> __device__ __forceinline__ float dpp(float x) {
  return __builtin_bit_cast(float, __builtin_amdgcn_mov_dpp(__builtin_bit_cast(int, x), CTRL, 0xf, 0xf, true));
}
constexpr int XOR1 = 0xB1, XOR2 = 0x4E, XOR7 = 0x141, ROR8 = 0x128, ROR4 = 0x124, BCAST = 0x150;
constexpr int QP0 = 0x00, QP1 = 0x55;
__device__ __forceinline__ float ex2(float x) { return __builtin_amdgcn_exp2f(x); }
__device__ __forceinline__ float rcp(float x) { return __builtin_amdgcn_rcpf(x); }

__device__ __forceinline__ float swap32_add(float a, float b) {
  auto s = __builtin_amdgcn_permlane32_swap(__float_as_uint(a), __float_as_uint(b), false, false);
  return __uint_as_float(s[0]) + __uint_as_float(s[1]);
}
__device__ __forceinline__ float swap16_add(float a, float b) {
  auto s = __builtin_amdgcn_permlane16_swap(__float_as_uint(a), __float_as_uint(b), false, false);
  return __uint_as_float(s[0]) + __uint_as_float(s[1]);
}
__device__ __forceinline__ float xrow_max(float x) {
  auto s = __builtin_amdgcn_permlane16_swap(__float_as_uint(x), __float_as_uint(x), false, false);
  x = fmaxf(__uint_as_float(s[0]), __uint_as_float(s[1]));
  auto t = __builtin_amdgcn_permlane32_swap(__float_as_uint(x), __float_as_uint(x), false, false);
  return fmaxf(__uint_as_float(t[0]), __uint_as_float(t[1]));
}
__device__ __forceinline__ float xrow_sum(float x) {
  x = swap16_add(x, x);
  return swap32_add(x, x);
}
__device__ __forceinline__ float row_sum(float x) {
  x += dpp<XOR1>(x);
  x += dpp<XOR2>(x);
  x += dpp<XOR7>(x);
  x += dpp<ROR8>(x);
  return x;
}
__device__ __forceinline__ float wave_sum(float x) { return xrow_sum(row_sum(x)); }
__device__ __forceinline__ float wave_treduce16(float (&v)[16], int lane) {
#pragma unroll
  for (int j = 0; j < 8; ++j) v[j] = swap32_add(v[j], v[j + 8]);
#pragma unroll
  for (int j = 0; j < 4; ++j) v[j] = swap16_add(v[j], v[j + 4]);
#pragma unroll
  for (int j = 0; j < 2; ++j) {
    const float a = v[j] + dpp<ROR8>(v[j]), b = v[j + 2] + dpp<ROR8>(v[j + 2]);
    v[j] = (lane & 8) ? b : a;
  }
  {
    const float a = v[0] + dpp<XOR7>(v[0]), b = v[1] + dpp<XOR7>(v[1]);
    v[0] = (lane & 4) ? b : a;
  }
  float r = v[0];
  r += dpp<XOR2>(r);
  r += dpp<XOR1>(r);
  return r;
}

#define SB() __builtin_amdgcn_sched_barrier(0)
#define PIN8(a) asm volatile("" : "+v"(a[0]), "+v"(a[1]), "+v"(a[2]), "+v"(a[3]), "+v"(a[4]), "+v"(a[5]), "+v"(a[6]), "+v"(a[7]) : : "memory")
#define LDS_BARRIER() do { asm volatile("s_waitcnt lgkmcnt(0)" ::: "memory"); __builtin_amdgcn_s_barrier(); asm volatile("" ::: "memory"); } while (0)

__global__ void __launch_bounds__(NTHR, 2)
fused_decode_kernel(const unsigned short* __restrict__ x, const unsigned short* __restrict__ kc, const unsigned short* __restrict__ vc,
                    const unsigned short* __restrict__ wq, const unsigned short* __restrict__ wo, const unsigned short* __restrict__ w1,
                    const unsigned short* __restrict__ b1, const unsigned short* __restrict__ w2, const unsigned short* __restrict__ b2,
                    unsigned short* __restrict__ out, unsigned char* ws) {
  __shared__ __attribute__((aligned(16))) unsigned lds[L_WORDS];
  float* ldf = reinterpret_cast<float*>(lds);
  const int tid = threadIdx.x, lane = tid & 63, w = __builtin_amdgcn_readfirstlane(tid >> 6);
  const int r = lane >> 4, c = lane & 15;
  const int bid = blockIdx.x, xcd = bid & 7, ii = bid >> 3;
  const int kvh = ii & 15, b = (xcd >> 1) & 3, sp = ((xcd & 1) << 1) | (ii >> 4);
  const int prod = b * 64 + kvh * 4 + sp;
  const int col0 = (prod & 63) * NOUT;
  const int h1 = 4 * xcd + (ii >> 3), cbk = ii & 7;

  const unsigned tag = *reinterpret_cast<const __attribute__((address_space(4))) unsigned*>(reinterpret_cast<uintptr_t>(ws + WS_EPOCH)) + TAG_STEP;
  const int lnb = w < 4 ? w : b;
  const __amdgpu_buffer_rsrc_t rx = __builtin_amdgcn_make_buffer_rsrc((void*)x, 0, NB * DM * 2, 0x00020000);
  const __amdgpu_buffer_rsrc_t rb2 = __builtin_amdgcn_make_buffer_rsrc((void*)b2, 0, 2, 0x00020000);
  unsigned short xr[9];
#pragma unroll
  for (int j = 0; j < 9; ++j) xr[j] = __builtin_amdgcn_raw_buffer_load_b16(rx, lane * 2 + j * 128, w <= 4 ? lnb * DM * 2 : 0x10000, 0);
  const unsigned short b2r = __builtin_amdgcn_raw_buffer_load_b16(rb2, 0, w == 4 ? 0 : 0x10000, 0);
  const size_t kvbase = ((size_t)(b * HKV + kvh) * SK + (size_t)sp * KEYS_WG) * DH;
  const __amdgpu_buffer_rsrc_t rk = __builtin_amdgcn_make_buffer_rsrc((void*)(kc + kvbase), 0, KEYS_WG * DH * 2, 0x00020000);
  const __amdgpu_buffer_rsrc_t rv = __builtin_amdgcn_make_buffer_rsrc((void*)(vc + kvbase), 0, KEYS_WG * DH * 2, 0x00020000);
  const __amdgpu_buffer_rsrc_t rwq = __builtin_amdgcn_make_buffer_rsrc((void*)wq, 0, DM * QN * 2, 0x00020000);
  const __amdgpu_buffer_rsrc_t rwo = __builtin_amdgcn_make_buffer_rsrc((void*)wo, 0, DM * QN * 2, 0x00020000);
  const __amdgpu_buffer_rsrc_t rs = __builtin_amdgcn_make_buffer_rsrc((void*)ws, 0, WS_BYTES, 0x00020000);
  constexpr int PBYTES = PAGE * DH * 2;
  constexpr int PSTR = NWAVE * PBYTES;
  const int lo16 = lane * 16;
  const int wpo = w * PBYTES;
#define LDK(i, t) __builtin_amdgcn_raw_buffer_load_b128(rk, lo16 + ((t) & 3) * 1024, wpo + (i) * PSTR + ((t) >> 2) * 4096, AUX_KV)
#define LDV(i, t) __builtin_amdgcn_raw_buffer_load_b128(rv, lo16 + ((t) & 3) * 1024, wpo + (i) * PSTR + ((t) >> 2) * 4096, AUX_KV)
  u32x4 kb0[8], vb0[8], kb1[8], vb1[8];
  const int cb = bid & 63, ks = bid >> 6;
  const int qrow = ks * 144 + w * 18 + (lane >> 3);
  u32x4 wqr[3];
  {
    const int wqv = (lane >> 3) * QN * 2 + (lane & 7) * 16;
    const int wqs = ((ks * 144 + w * 18) * QN + cb * 64) * 2;
    wqr[0] = __builtin_amdgcn_raw_buffer_load_b128(rwq, wqv, wqs, AUX_NT);
    wqr[1] = __builtin_amdgcn_raw_buffer_load_b128(rwq, wqv, wqs + 8 * QN * 2, AUX_NT);
    wqr[2] = __builtin_amdgcn_raw_buffer_load_b128(rwq, lane < 16 ? wqv : 0x7fffffff, wqs + 16 * QN * 2, AUX_NT);
  }
  SB();
  asm volatile("" ::: "memory");
  __builtin_amdgcn_s_barrier();
  asm volatile("" ::: "memory");
  SB();
#pragma unroll
  for (int t = 0; t < 8; ++t) { kb0[t] = LDK(0, t); SB(); }
  asm volatile("" :: "v"(xr[0]), "v"(xr[1]), "v"(xr[2]), "v"(xr[3]), "v"(xr[4]), "v"(xr[5]), "v"(xr[6]), "v"(xr[7]), "v"(xr[8]), "v"(b2r));
  SB();

  if (w <= 4) {
    float xv[9];
    float s = 0.f;
#pragma unroll
    for (int j = 0; j < 9; ++j) { xv[j] = bf2f(xr[j]); s += xv[j]; }
    if (w == 4) {
#pragma unroll
      for (int j = 0; j < 9; ++j) ldf[L_X + j * 64 + lane] = xv[j];
      ldf[L_FLAG + 2] = bf2f(b2r);
    }
    const float mean = wave_sum(s) * (1.0f / DM);
    float v = 0.f;
#pragma unroll
    for (int j = 0; j < 9; ++j) { xv[j] -= mean; v = fmaf(xv[j], xv[j], v); }
    const float rstd = 1.0f / sqrtf(wave_sum(v) * (1.0f / DM) + EPS);
    if (w < 4) {
#pragma unroll
      for (int j = 0; j < 9; ++j) lds[L_XN + (j * 64 + lane) * 4 + w] = cvtpk(xv[j] * rstd, 0.f) & 0xffffu;
    } else {
      float s2 = 0.f;
#pragma unroll
      for (int j = 0; j < 9; ++j) { xv[j] = bf_lo(cvtpk(xv[j] * rstd, 0.f)); s2 += xv[j]; }
      const float mean2 = wave_sum(s2) * (1.0f / DM);
      float v2 = 0.f;
#pragma unroll
      for (int j = 0; j < 9; ++j) { xv[j] -= mean2; v2 = fmaf(xv[j], xv[j], v2); }
      const float rstd2 = 1.0f / sqrtf(wave_sum(v2) * (1.0f / DM) + EPS);
#pragma unroll
      for (int j = 0; j < 9; ++j) ldf[L_H + j * 64 + lane] = xv[j] * rstd2;
    }
  } else if (w == 5) {
    if (lane < 2) lds[L_FLAG + lane] = 0u;
  }
  LDS_BARRIER();
  {
    float qa[32];
#pragma unroll
    for (int k = 0; k < 32; ++k) qa[k] = 0.f;
#pragma unroll
    for (int j = 0; j < 3; ++j) {
      const int row = (j < 2) ? qrow + 8 * j : min(qrow + 16, DM - 1);
      const u32x4 xq = *reinterpret_cast<const u32x4*>(lds + L_XN + row * 4);
#pragma unroll
      for (int bb = 0; bb < 4; ++bb) {
        const unsigned xl = xq[bb], xh = xq[bb] << 16;
#pragma unroll
        for (int k = 0; k < 4; ++k) {
          qa[bb * 8 + 2 * k] = dot2(wqr[j][k], xl, qa[bb * 8 + 2 * k]);
          qa[bb * 8 + 2 * k + 1] = dot2(wqr[j][k], xh, qa[bb * 8 + 2 * k + 1]);
        }
      }
    }
#pragma unroll
    for (int k = 0; k < 16; ++k) qa[k] = swap32_add(qa[k], qa[k + 16]);
#pragma unroll
    for (int k = 0; k < 8; ++k) qa[k] = swap16_add(qa[k], qa[k + 8]);
    f32x4 qs;
#pragma unroll
    for (int k = 0; k < 4; ++k) {
      const float t1 = qa[k] + dpp<ROR8>(qa[k]), t2 = qa[k + 4] + dpp<ROR8>(qa[k + 4]);
      qs[k] = (lane & 8) ? t2 : t1;
    }
    *reinterpret_cast<f32x4*>(ldf + L_QP + w * 256 + (lane >> 4) * 64 + (lane & 7) * 8 + ((lane >> 3) & 1) * 4) = qs;
  }
  LDS_BARRIER();
  if (w < 2) {
    float q0 = 0.f, q1 = 0.f;
#pragma unroll
    for (int ww = 0; ww < NWAVE; ++ww) {
      const float2 t = *reinterpret_cast<const float2*>(ldf + L_QP + ww * 256 + tid * 2);
      q0 += t.x; q1 += t.y;
    }
    const u32x4 gq = {tag, __float_as_uint(q0), tag, __float_as_uint(q1)};
    __builtin_amdgcn_raw_buffer_store_b128(gq, rs, WS_QG + (((tid >> 5) * 4 + ks) * QN + cb * 64 + (tid & 31) * 2) * 8, 0, AUX_SC1);
    const int qo = WS_QG + ((b * 4) * QN + kvh * 256 + tid * 2) * 8;
    u32x4 gv[4];
    bool ok = false;
#pragma unroll 1
    for (unsigned spins = 0; spins < SPIN_CAP; ++spins) {
      if (!ok) {
#pragma unroll
        for (int s = 0; s < 4; ++s) gv[s] = __builtin_amdgcn_raw_buffer_load_b128(rs, qo, s * QN * 8, AUX_SC1);
        ok = true;
#pragma unroll
        for (int s = 0; s < 4; ++s) ok &= (gv[s][0] == tag) & (gv[s][2] == tag);
      }
      if (__all(ok)) break;
      asm volatile("" ::: "memory");
    }
    q0 = (__uint_as_float(gv[0][1]) + __uint_as_float(gv[1][1])) + (__uint_as_float(gv[2][1]) + __uint_as_float(gv[3][1]));
    q1 = (__uint_as_float(gv[0][3]) + __uint_as_float(gv[1][3])) + (__uint_as_float(gv[2][3]) + __uint_as_float(gv[3][3]));
    lds[L_QB + tid] = cvtpk(q0, q1);
    asm volatile("" ::: "memory");
    if (lane == 0) __hip_atomic_store(lds + L_FLAG + w, 1u, __ATOMIC_RELAXED, __HIP_MEMORY_SCOPE_WORKGROUP);
    asm volatile("" ::: "memory");
  }
  auto q_ready = [&]() {
    const unsigned long long f = __hip_atomic_load(reinterpret_cast<unsigned long long*>(lds + L_FLAG), __ATOMIC_RELAXED, __HIP_MEMORY_SCOPE_WORKGROUP);
    return __builtin_amdgcn_readfirstlane((unsigned)f & (unsigned)(f >> 32)) != 0;
  };
  bool ready = false;
#define CHUNK(dst, LD, pg, half) do { SB(); _Pragma("unroll") for (int t = 4 * (half); t < 4 * (half) + 4; ++t) dst[t] = LD(pg, t); SB(); \
    if (!ready) { asm volatile("s_waitcnt vmcnt(4)" ::: "memory"); ready = q_ready(); } SB(); } while (0)
  CHUNK(vb0, LDV, 0, 0);
  CHUNK(vb0, LDV, 0, 1);
  CHUNK(kb1, LDK, 1, 0);
  CHUNK(kb1, LDK, 1, 1);
  CHUNK(vb1, LDV, 1, 0);
#pragma unroll
  for (int t = 4; t < 8; ++t) vb1[t] = LDV(1, t);
  SB();
  u32x4 mw[4], wt[3];
#pragma unroll
  for (int j = 0; j < 3; ++j) {
    const unsigned short* src = j == 0 ? w1 : (j == 1 ? b1 : w2);
    mw[j] = reinterpret_cast<const u32x4*>(src)[72 * w + lane];
  }
  {
    const int a = lane >> 3;
    const unsigned short* src = a == 0 ? w1 : (a == 1 ? b1 : w2);
    mw[3] = reinterpret_cast<const u32x4*>(src)[72 * w + 64 + (lane & 7)];
  }
#pragma unroll
  for (int j = 0; j < 3; ++j) {
    const int ch = tid + NTHR * j;
    const int row = (ch * 7282) >> 16;
    const int vo = row * (DM * 2) + (ch - row * 9) * 16;
    wt[j] = __builtin_amdgcn_raw_buffer_load_b128(rwo, (j < 2 || tid < 128) ? vo : 0x7fffffff, (h1 * DH * DM + cbk * CB) * 2, AUX_NT);
  }
  SB();
  while (!ready) { __builtin_amdgcn_s_sleep(1); ready = q_ready(); }
  asm volatile("" ::: "memory");
  const u32x4 qh0 = *reinterpret_cast<const u32x4*>(lds + L_QB + c * 4);
  const u32x4 qh1 = *reinterpret_cast<const u32x4*>(lds + L_QB + 64 + c * 4);
  float hv[NOUT];
  float ya[16];
#pragma unroll
  for (int j = 0; j < 16; ++j) ya[j] = 0.f;
#pragma unroll
  for (int j = 0; j < NOUT; ++j) hv[j] = __builtin_bit_cast(float, __builtin_amdgcn_readfirstlane(lds[L_H + col0 + j]));
  const unsigned short* mwl = reinterpret_cast<const unsigned short*>(lds + L_MW + w * MW_W);

  auto mlp_slice = [&](int k) {
    const int f = k * 64 + lane;
    const float w1f = bf2f(mwl[f]), b1f = bf2f(mwl[576 + f]), w2f = bf2f(mwl[2 * 576 + f]);
    constexpr float C1 = -2.0f * 0.7978845608028654f * LOG2E, C2 = C1 * 0.044715f;
#pragma unroll
    for (int j = 0; j < NOUT; ++j) {
      const float uu = fmaf(hv[j], w1f, b1f);
      const float e = ex2(uu * fmaf(uu * uu, C2, C1));
      ya[j] = fmaf(uu * w2f, rcp(1.0f + e), ya[j]);
    }
  };

  float m = -INFINITY, l = 0.f;
  float a0[8], a1[8];
#pragma unroll
  for (int k = 0; k < 8; ++k) { a0[k] = 0.f; a1[k] = 0.f; }

#define ISSUE(N, so, first) do { if (!(first) || (N) >= 8) { \
      _Pragma("unroll") for (int t = 0; t < 4; ++t) { \
        const int o_ = ((N) < 8 ? (so) : so_n) + (((N) >> 2) & 1) * PSTR + ((N) & 1) * 4096; \
        if (((N) & 6) == 0) kb0[4 * ((N) & 1) + t] = __builtin_amdgcn_raw_buffer_load_b128(rk, lo16 + t * 1024, o_, AUX_KV); \
        if (((N) & 6) == 2) vb0[4 * ((N) & 1) + t] = __builtin_amdgcn_raw_buffer_load_b128(rv, lo16 + t * 1024, o_, AUX_KV); \
        if (((N) & 6) == 4) kb1[4 * ((N) & 1) + t] = __builtin_amdgcn_raw_buffer_load_b128(rk, lo16 + t * 1024, o_, AUX_KV); \
        if (((N) & 6) == 6) vb1[4 * ((N) & 1) + t] = __builtin_amdgcn_raw_buffer_load_b128(rv, lo16 + t * 1024, o_, AUX_KV); \
        SB(); } } } while (0)
#define QK_HALF(kb, hf) _Pragma("unroll") for (int t = 4 * (hf); t < 4 * (hf) + 4; ++t) { \
      float d0 = 0.f, d1 = 0.f; \
      _Pragma("unroll") for (int k = 0; k < 4; ++k) { d0 = dot2(kb[t][k], qh0[k], d0); d1 = dot2(kb[t][k], qh1[k], d1); } \
      xs[2 * t] = d0; xs[2 * t + 1] = d1; }
#define TR_STEP(HALF, CTRL, BIT) _Pragma("unroll") for (int h = 0; h < (HALF); ++h) { \
      const float t1_ = xs[h] + dpp<CTRL>(xs[h]), t2_ = xs[h + (HALF)] + dpp<CTRL>(xs[h + (HALF)]); \
      xs[h] = (c & (BIT)) ? t2_ : t1_; }
#define PV_STEP(vb, t) { const float p0 = dpp<BCAST + 2 * (t)>(p), p1 = dpp<BCAST + 2 * (t) + 1>(p); \
      _Pragma("unroll") for (int k = 0; k < 4; ++k) { const float lo = bf_lo(vb[t][k]), hi = bf_hi(vb[t][k]); \
        a0[2 * k] = fmaf(p0, lo, a0[2 * k]); a0[2 * k + 1] = fmaf(p0, hi, a0[2 * k + 1]); \
        a1[2 * k] = fmaf(p1, lo, a1[2 * k]); a1[2 * k + 1] = fmaf(p1, hi, a1[2 * k + 1]); } }
#define PAGE_STEP(kb, vb, S, so, first) do { \
    float xs[16]; \
    QK_HALF(kb, 0) \
    PIN8(xs); SB(); \
    ISSUE((S) + DEPTH, so, first); \
    QK_HALF(kb, 1) \
    PIN8((xs + 8)); SB(); \
    ISSUE((S) + 1 + DEPTH, so, first); \
    TR_STEP(8, ROR8, 8) TR_STEP(4, XOR7, 4) TR_STEP(2, XOR2, 2) TR_STEP(1, XOR1, 1) \
    const float sc = xs[0] * SC2; \
    float pm = fmaxf(sc, dpp<XOR2>(sc)); \
    pm = fmaxf(pm, dpp<ROR4>(pm)); \
    pm = fmaxf(pm, dpp<ROR8>(pm)); \
    const float mn = fmaxf(m, pm); \
    const float alpha = ex2(m - mn), p = ex2(sc - mn); \
    l = fmaf(l, alpha, p); \
    m = mn; \
    const float al0 = dpp<QP0>(alpha), al1 = dpp<QP1>(alpha); \
    _Pragma("unroll") for (int k = 0; k < 8; ++k) { a0[k] *= al0; a1[k] *= al1; } \
    PV_STEP(vb, 0) PV_STEP(vb, 1) PV_STEP(vb, 2) PV_STEP(vb, 3) \
    PIN8(a0); PIN8(a1); SB(); \
    ISSUE((S) + 2 + DEPTH, so, first); \
    PV_STEP(vb, 4) PV_STEP(vb, 5) PV_STEP(vb, 6) PV_STEP(vb, 7) \
    PIN8(a0); PIN8(a1); SB(); \
    ISSUE((S) + 3 + DEPTH, so, first); \
  } while (0)
  int so_n = wpo + 2 * PSTR;
  PAGE_STEP(kb0, vb0, 0, wpo, true);
  PAGE_STEP(kb1, vb1, 4, wpo, true);
  {
    unsigned* mwd = lds + L_MW + w * MW_W;
#pragma unroll
    for (int j = 0; j < 3; ++j) *reinterpret_cast<u32x4*>(mwd + j * 288 + lane * 4) = mw[j];
    if (lane < 24) *reinterpret_cast<u32x4*>(mwd + (lane >> 3) * 288 + 256 + (lane & 7) * 4) = mw[3];
#pragma unroll
    for (int j = 0; j < 2; ++j) *reinterpret_cast<u32x4*>(lds + L_WT + (tid + NTHR * j) * 4) = wt[j];
    if (tid < 128) *reinterpret_cast<u32x4*>(lds + L_WT + (tid + NTHR * 2) * 4) = wt[2];
    asm volatile("" ::: "memory");
    mlp_slice(0);
    mlp_slice(1);
    mlp_slice(2);
  }
  SB();
#pragma unroll 1
  for (int i = 2; i < NPG; i += 2) {
    const int so = so_n;
    so_n = so + 2 * PSTR;
    mlp_slice(i + 1);
    SB();
    PAGE_STEP(kb0, vb0, 0, so, false);
    mlp_slice(i + 2);
    SB();
    PAGE_STEP(kb1, vb1, 4, so, false);
  }

  {
    const float mw_ = xrow_max(m);
    const float sc_ = ex2(m - mw_);
    float lr = l * sc_;
    lr += dpp<XOR2>(lr);
    lr += dpp<ROR4>(lr);
    lr += dpp<ROR8>(lr);
    lr = xrow_sum(lr);
    const float s0 = dpp<QP0>(sc_), s1 = dpp<QP1>(sc_);
    float t8[8];
#pragma unroll
    for (int k = 0; k < 8; ++k) t8[k] = swap32_add(a0[k] * s0, a1[k] * s1);
    f32x4 t4;
#pragma unroll
    for (int k = 0; k < 4; ++k) t4[k] = swap16_add(t8[k], t8[k + 4]);
    float* rec = ldf + L_MW + w * MW_W;
    *reinterpret_cast<f32x4*>(rec + (lane >> 5) * 128 + c * 8 + (r & 1) * 4) = t4;
    if (lane < 2) { rec[256 + lane * 2] = mw_; rec[257 + lane * 2] = lr; }
  }
  LDS_BARRIER();
  if (w < 2) {
    float mm[NWAVE], ll[NWAVE];
    float M = -INFINITY;
#pragma unroll
    for (int ww = 0; ww < NWAVE; ++ww) {
      const float2 t = *reinterpret_cast<const float2*>(ldf + L_MW + ww * MW_W + 256 + w * 2);
      mm[ww] = t.x; ll[ww] = t.y; M = fmaxf(M, t.x);
    }
    float O0 = 0.f, O1 = 0.f, L = 0.f;
#pragma unroll
    for (int ww = 0; ww < NWAVE; ++ww) {
      const float wgt = ex2(mm[ww] - M);
      L = fmaf(wgt, ll[ww], L);
      const float2 t = *reinterpret_cast<const float2*>(ldf + L_MW + ww * MW_W + w * 128 + lane * 2);
      O0 = fmaf(wgt, t.x, O0); O1 = fmaf(wgt, t.y, O1);
    }
    const int ro = WS_OG + (prod * 2 + w) * OREC;
    const u32x4 go = {tag, __float_as_uint(O0), tag, __float_as_uint(O1)};
    __builtin_amdgcn_raw_buffer_store_b128(go, rs, lo16, ro, AUX_SC1);
    const u32x4 gm = {tag, __float_as_uint(M), tag, __float_as_uint(L)};
    __builtin_amdgcn_raw_buffer_store_b128(gm, rs, lane == 0 ? 1024 : 0x7fffffff, ro, AUX_SC1);
  }
  {
    const int ro = WS_OG + (((w >> 1) * 64 + (h1 >> 1) * 4) * 2 + (h1 & 1)) * OREC;
    const int vo = r * 2 * OREC + ((w & 1) * 64 + c * 4) * 8;
    u32x4 g0, g1, gm;
    g0 = __builtin_amdgcn_raw_buffer_load_b128(rs, vo, ro, AUX_SC1);
    g1 = __builtin_amdgcn_raw_buffer_load_b128(rs, vo + 16, ro, AUX_SC1);
    gm = __builtin_amdgcn_raw_buffer_load_b128(rs, r * 2 * OREC + 1024, ro, AUX_SC1);
    SB();
    const float ysum = wave_treduce16(ya, lane);
    if ((lane & 3) == 0) ldf[L_Y + w * 16 + (lane >> 2)] = ysum;
    u32x4 bfr[4];
    if (w < 5) {
      const unsigned short* wts = reinterpret_cast<const unsigned short*>(lds + L_WT) + (r * 8) * CB + w * 16 + c;
#pragma unroll
      for (int kk = 0; kk < 4; ++kk)
#pragma unroll
        for (int j = 0; j < 4; ++j)
          bfr[kk][j] = (unsigned)wts[(kk * 32 + 2 * j) * CB] | ((unsigned)wts[(kk * 32 + 2 * j + 1) * CB] << 16);
    }
    SB();
    bool ok = (g0[0] == tag) & (g0[2] == tag) & (g1[0] == tag) & (g1[2] == tag) & (gm[0] == tag) & (gm[2] == tag);
#pragma unroll 1
    for (unsigned spins = 0; spins < SPIN_CAP; ++spins) {
      const unsigned long long miss = __ballot(!ok);
      if (miss == 0) break;
      if (__popcll(miss) > 16) __builtin_amdgcn_s_sleep(16);
      if (!ok) {
        g0 = __builtin_amdgcn_raw_buffer_load_b128(rs, vo, ro, AUX_SC1);
        g1 = __builtin_amdgcn_raw_buffer_load_b128(rs, vo + 16, ro, AUX_SC1);
        gm = __builtin_amdgcn_raw_buffer_load_b128(rs, r * 2 * OREC + 1024, ro, AUX_SC1);
        ok = (g0[0] == tag) & (g0[2] == tag) & (g1[0] == tag) & (g1[2] == tag) & (gm[0] == tag) & (gm[2] == tag);
      }
      asm volatile("" ::: "memory");
    }
    const float Ms = __uint_as_float(gm[1]);
    const float wgt = ex2(Ms - xrow_max(Ms));
    const float Ls = xrow_sum(wgt * __uint_as_float(gm[3]));
    const float t0 = swap32_add(wgt * __uint_as_float(g0[1]), wgt * __uint_as_float(g1[1]));
    const float t1 = swap32_add(wgt * __uint_as_float(g0[3]), wgt * __uint_as_float(g1[3]));
    const float ov = swap16_add(t0, t1) * rcp(Ls);
    reinterpret_cast<unsigned short*>(lds + L_O)[(w >> 1) * DH + (w & 1) * 64 + c * 4 + r] = (unsigned short)(cvtpk(ov, 0.f) & 0xffffu);
    LDS_BARRIER();
    if (w < 5) {
      f32x4 acc0 = {0.f, 0.f, 0.f, 0.f}, acc1 = {0.f, 0.f, 0.f, 0.f};
      u32x4 afr[4];
#pragma unroll
      for (int kk = 0; kk < 4; ++kk) afr[kk] = *reinterpret_cast<const u32x4*>(lds + L_O + (c & 3) * (DH / 2) + kk * 16 + r * 4);
      acc0 = __builtin_amdgcn_mfma_f32_16x16x32_bf16(__builtin_bit_cast(bf16x8, bfr[0]), __builtin_bit_cast(bf16x8, afr[0]), acc0, 0, 0, 0);
      acc1 = __builtin_amdgcn_mfma_f32_16x16x32_bf16(__builtin_bit_cast(bf16x8, bfr[1]), __builtin_bit_cast(bf16x8, afr[1]), acc1, 0, 0, 0);
      acc0 = __builtin_amdgcn_mfma_f32_16x16x32_bf16(__builtin_bit_cast(bf16x8, bfr[2]), __builtin_bit_cast(bf16x8, afr[2]), acc0, 0, 0, 0);
      acc1 = __builtin_amdgcn_mfma_f32_16x16x32_bf16(__builtin_bit_cast(bf16x8, bfr[3]), __builtin_bit_cast(bf16x8, afr[3]), acc1, 0, 0, 0);
      const int so = (c < 4 && w * 16 + r * 4 < CB) ? c * SROW + r * 32 : 0x7fffffff;
      const int sb = WS_SG + (h1 * 8 + cbk) * SREC + w * 128;
      const u32x4 s0 = {tag, __float_as_uint(acc0[0] + acc1[0]), tag, __float_as_uint(acc0[1] + acc1[1])};
      const u32x4 s1 = {tag, __float_as_uint(acc0[2] + acc1[2]), tag, __float_as_uint(acc0[3] + acc1[3])};
      __builtin_amdgcn_raw_buffer_store_b128(s0, rs, so, sb, AUX_SC1);
      __builtin_amdgcn_raw_buffer_store_b128(s1, rs, so, sb + 16, AUX_SC1);
    } else {
      const int odd = prod & 1, pr = 2 * (w - 5) + (r >> 1);
      const int j0 = 2 * pr - odd;
      const bool v0 = j0 >= 0 && j0 < NOUT, v1 = j0 + 1 < NOUT;
      const int so = WS_SG + ((prod & 63) >> 3) * SREC + b * SROW + ((prod & 7) * NOUT - odd) * 8;
      const int vo2 = pr < 5 ? ((r & 1) * 16 + c) * 8 * SREC + pr * 16 : 0x7fffffff;
      u32x4 gs = __builtin_amdgcn_raw_buffer_load_b128(rs, vo2, so, AUX_SC1);
      SB();
      const int jj = min(max(j0 + (c & 1), 0), NOUT - 1);
      float y = ldf[L_FLAG + 2];
#pragma unroll
      for (int ww = 0; ww < NWAVE; ++ww) y += ldf[L_Y + ww * 16 + jj];
      const float g = y / (1.0f + __expf(-y));
      const float xo = ldf[L_X + col0 + jj];
      SB();
      bool ok2 = (pr >= 5) | ((gs[0] == tag) & (gs[2] == tag));
#pragma unroll 1
      for (unsigned spins = 0; spins < SPIN_CAP; ++spins) {
        if (__all(ok2)) break;
        if (!ok2) {
          gs = __builtin_amdgcn_raw_buffer_load_b128(rs, vo2, so, AUX_SC1);
          ok2 = (gs[0] == tag) & (gs[2] == tag);
        }
        asm volatile("" ::: "memory");
      }
      const float e0 = __uint_as_float(gs[1]), e1 = __uint_as_float(gs[3]);
      const float u0 = e0 + dpp<XOR1>(e0), u1 = e1 + dpp<XOR1>(e1);
      float s = (c & 1) ? u1 : u0;
      s += dpp<XOR2>(s);
      s += dpp<ROR4>(s);
      s += dpp<ROR8>(s);
      s = swap16_add(s, s);
      if ((r & 1) == 0 && c < 2 && (c ? v1 : v0)) out[b * DM + col0 + j0 + c] = (unsigned short)(cvtpk(fmaf(s, g, xo), 0.f) & 0xffffu);
      if (bid == 0 && w == 5 && lane == 0) __hip_atomic_store(reinterpret_cast<unsigned*>(ws + WS_EPOCH), tag, RLX_AGENT);
    }
  }
}

}

extern "C" void kernel_launch(void* const* d_in, const int* in_sizes, int n_in, void* d_out, int out_size, void* d_ws, size_t ws_size,
                              hipStream_t stream) {
  (void)in_sizes; (void)n_in; (void)out_size; (void)ws_size;
  fused_decode_kernel<<<NWG, NTHR, 0, stream>>>(
      (const unsigned short*)d_in[0], (const unsigned short*)d_in[1], (const unsigned short*)d_in[2], (const unsigned short*)d_in[3],
      (const unsigned short*)d_in[4], (const unsigned short*)d_in[6], (const unsigned short*)d_in[7], (const unsigned short*)d_in[8],
      (const unsigned short*)d_in[9], (unsigned short*)d_out, (unsigned char*)d_ws);
}
```

```cpp
#include <hip/hip_runtime.h>
#include <hip/hip_bf16.h>
#include <stdint.h>

#define ONEST 1
#define OUTAUX -1
namespace {

constexpr int NB = 4, DM = 576, HQ = 32, HKV = 16, SK = 8192, DH = 128, FF = 4608;
constexpr int NWG = 256, NTHR = 512, NWAVE = 8;
constexpr int NSPLIT = 4;
constexpr int KEYS_WG = SK / NSPLIT;
constexpr int PAGE = 32;
constexpr int NPG = KEYS_WG / PAGE / NWAVE;
constexpr int QN = HQ * DH;
constexpr float LOG2E = 1.4426950408889634f;
constexpr float SC2 = 0.08838834764831845f * LOG2E;
constexpr float EPS = 1e-5f;
constexpr int DEPTH = 2;
constexpr int CB = 72;
constexpr int NOUT = 9;

constexpr int WS_EPOCH = 0;
constexpr int WS_QG = 4096;
constexpr int WS_OG = WS_QG + NB * 4 * QN * 8;
constexpr int OREC = 1152;
constexpr int WS_SG = WS_OG + NWG * 2 * OREC;
constexpr int SROW = 640, SREC = NB * SROW;
constexpr int WS_BYTES = WS_SG + HQ * 8 * SREC;
constexpr unsigned TAG_STEP = 0x9E3779B1u;
constexpr unsigned SPIN_CAP = 1u << 20;

constexpr int L_XN = 0;
constexpr int L_QP = L_XN + DM * 4;
constexpr int L_QB = L_QP + NWAVE * 256;
constexpr int L_FLAG = L_QB + 128;
constexpr int L_H = L_FLAG + 4;
constexpr int L_X = L_H + DM;
constexpr int L_MW = L_X + DM;
constexpr int MW_W = 3 * 288;
constexpr int L_WT = L_MW + NWAVE * MW_W;
constexpr int L_O = L_WT + DH * CB / 2 + 8;
constexpr int L_Y = L_O + NB * DH / 2;
constexpr int L_WORDS = L_Y + NWAVE * 16;
static_assert(L_WORDS * 4 <= 163840, "LDS");
static_assert((L_QP % 4 | L_QB % 4 | L_MW % 4 | L_WT % 4 | L_O % 4 | L_FLAG % 2) == 0, "align");

typedef unsigned u32x4 __attribute__((ext_vector_type(4)));
typedef unsigned u32x2 __attribute__((ext_vector_type(2)));
typedef float f32x4 __attribute__((ext_vector_type(4)));
typedef __bf16 bf16x2 __attribute__((ext_vector_type(2)));
typedef __bf16 bf16x8 __attribute__((ext_vector_type(8)));
#define RLX_AGENT __ATOMIC_RELAXED, __HIP_MEMORY_SCOPE_AGENT
constexpr int AUX_SC1 = 16, AUX_NT = 2, AUX_KV = 18;

__device__ __forceinline__ float dot2(unsigned a, unsigned b, float acc) {
  return __builtin_amdgcn_fdot2_f32_bf16(__builtin_bit_cast(bf16x2, a), __builtin_bit_cast(bf16x2, b), acc, false);
}
__device__ __forceinline__ unsigned cvtpk(float lo, float hi) {
  bf16x2 r = {(__bf16)lo, (__bf16)hi};
  return __builtin_bit_cast(unsigned, r);
}
__device__ __forceinline__ float bf_lo(unsigned v) { return __builtin_bit_cast(float, v << 16); }
__device__ __forceinline__ float bf_hi(unsigned v) { return __builtin_bit_cast(float, v & 0xffff0000u); }
__device__ __forceinline__ float bf2f(unsigned short v) { return __builtin_bit_cast(float, (unsigned)v << 16); }
template <int CTRL> __device__ __forceinline__ float dpp(float x) {
  return __builtin_bit_cast(float, __builtin_amdgcn_mov_dpp(__builtin_bit_cast(int, x), CTRL, 0xf, 0xf, true));
}
constexpr int XOR1 = 0xB1, XOR2 = 0x4E, XOR7 = 0x141, ROR8 = 0x128, ROR4 = 0x124, BCAST = 0x150;
constexpr int QP0 = 0x00, QP1 = 0x55;
__device__ __forceinline__ float ex2(float x) { return __builtin_amdgcn_exp2f(x); }
__device__ __forceinline__ float rcp(float x) { return __builtin_amdgcn_rcpf(x); }

__device__ __forceinline__ float swap32_add(float a, float b) {
  auto s = __builtin_amdgcn_permlane32_swap(__float_as_uint(a), __float_as_uint(b), false, false);
  return __uint_as_float(s[0]) + __uint_as_float(s[1]);
}
__device__ __forceinline__ float swap16_add(float a, float b) {
  auto s = __builtin_amdgcn_permlane16_swap(__float_as_uint(a), __float_as_uint(b), false, false);
  return __uint_as_float(s[0]) + __uint_as_float(s[1]);
}
__device__ __forceinline__ float xrow_max(float x) {
  auto s = __builtin_amdgcn_permlane16_swap(__float_as_uint(x), __float_as_uint(x), false, false);
  x = fmaxf(__uint_as_float(s[0]), __uint_as_float(s[1]));
  auto t = __builtin_amdgcn_permlane32_swap(__float_as_uint(x), __float_as_uint(x), false, false);
  return fmaxf(__uint_as_float(t[0]), __uint_as_float(t[1]));
}
__device__ __forceinline__ float xrow_sum(float x) {
  x = swap16_add(x, x);
  return swap32_add(x, x);
}
__device__ __forceinline__ float row_sum(float x) {
  x += dpp<XOR1>(x);
  x += dpp<XOR2>(x);
  x += dpp<XOR7>(x);
  x += dpp<ROR8>(x);
  return x;
}
__device__ __forceinline__ float wave_sum(float x) { return xrow_sum(row_sum(x)); }
__device__ __forceinline__ float wave_treduce16(float (&v)[16], int lane) {
#pragma unroll
  for (int j = 0; j < 8; ++j) v[j] = swap32_add(v[j], v[j + 8]);
#pragma unroll
  for (int j = 0; j < 4; ++j) v[j] = swap16_add(v[j], v[j + 4]);
#pragma unroll
  for (int j = 0; j < 2; ++j) {
    const float a = v[j] + dpp<ROR8>(v[j]), b = v[j + 2] + dpp<ROR8>(v[j + 2]);
    v[j] = (lane & 8) ? b : a;
  }
  {
    const float a = v[0] + dpp<XOR7>(v[0]), b = v[1] + dpp<XOR7>(v[1]);
    v[0] = (lane & 4) ? b : a;
  }
  float r = v[0];
  r += dpp<XOR2>(r);
  r += dpp<XOR1>(r);
  return r;
}

#define SB() __builtin_amdgcn_sched_barrier(0)
#define PIN8(a) asm volatile("" : "+v"(a[0]), "+v"(a[1]), "+v"(a[2]), "+v"(a[3]), "+v"(a[4]), "+v"(a[5]), "+v"(a[6]), "+v"(a[7]) : : "memory")
#define LDS_BARRIER() do { asm volatile("s_waitcnt lgkmcnt(0)" ::: "memory"); __builtin_amdgcn_s_barrier(); asm volatile("" ::: "memory"); } while (0)

__global__ void __launch_bounds__(NTHR, 2)
fused_decode_kernel(const unsigned short* __restrict__ x, const unsigned short* __restrict__ kc, const unsigned short* __restrict__ vc,
                    const unsigned short* __restrict__ wq, const unsigned short* __restrict__ wo, const unsigned short* __restrict__ w1,
                    const unsigned short* __restrict__ b1, const unsigned short* __restrict__ w2, const unsigned short* __restrict__ b2,
                    unsigned short* __restrict__ out, unsigned char* ws) {
  __shared__ __attribute__((aligned(16))) unsigned lds[L_WORDS];
  float* ldf = reinterpret_cast<float*>(lds);
  const int tid = threadIdx.x, lane = tid & 63, w = __builtin_amdgcn_readfirstlane(tid >> 6);
  const int r = lane >> 4, c = lane & 15;
  const int bid = blockIdx.x, xcd = bid & 7, ii = bid >> 3;
  const int kvh = ii & 15, b = (xcd >> 1) & 3, sp = ((xcd & 1) << 1) | (ii >> 4);
  const int prod = b * 64 + kvh * 4 + sp;
  const int col0 = (prod & 63) * NOUT;
  const int h1 = 4 * xcd + (ii >> 3), cbk = ii & 7;

  const unsigned tag = *reinterpret_cast<const __attribute__((address_space(4))) unsigned*>(reinterpret_cast<uintptr_t>(ws + WS_EPOCH)) + TAG_STEP;
  const int lnb = w < 4 ? w : b;
  const __amdgpu_buffer_rsrc_t rx = __builtin_amdgcn_make_buffer_rsrc((void*)x, 0, NB * DM * 2, 0x00020000);
  const __amdgpu_buffer_rsrc_t rb2 = __builtin_amdgcn_make_buffer_rsrc((void*)b2, 0, 2, 0x00020000);
  unsigned short xr[9];
#pragma unroll
  for (int j = 0; j < 9; ++j) xr[j] = __builtin_amdgcn_raw_buffer_load_b16(rx, lane * 2 + j * 128, w <= 4 ? lnb * DM * 2 : 0x10000, 0);
  const unsigned short b2r = __builtin_amdgcn_raw_buffer_load_b16(rb2, 0, w == 4 ? 0 : 0x10000, 0);
  const size_t kvbase = ((size_t)(b * HKV + kvh) * SK + (size_t)sp * KEYS_WG) * DH;
  const __amdgpu_buffer_rsrc_t rk = __builtin_amdgcn_make_buffer_rsrc((void*)(kc + kvbase), 0, KEYS_WG * DH * 2, 0x00020000);
  const __amdgpu_buffer_rsrc_t rv = __builtin_amdgcn_make_buffer_rsrc((void*)(vc + kvbase), 0, KEYS_WG * DH * 2, 0x00020000);
  const __amdgpu_buffer_rsrc_t rwq = __builtin_amdgcn_make_buffer_rsrc((void*)wq, 0, DM * QN * 2, 0x00020000);
  const __amdgpu_buffer_rsrc_t rwo = __builtin_amdgcn_make_buffer_rsrc((void*)wo, 0, DM * QN * 2, 0x00020000);
  const __amdgpu_buffer_rsrc_t rs = __builtin_amdgcn_make_buffer_rsrc((void*)ws, 0, WS_BYTES, 0x00020000);
  constexpr int PBYTES = PAGE * DH * 2;
  constexpr int PSTR = NWAVE * PBYTES;
  const int lo16 = lane * 16;
  const int wpo = w * PBYTES;
#define LDC(rs_, n, t) __builtin_amdgcn_raw_buffer_load_b128(rs_, lo16 + (t) * 1024, wpo + ((n) >> 1) * PSTR + ((n) & 1) * 4096, AUX_KV)
  u32x4 cb0[4], cb1[4], cb2[4], cb3[4], cb4[4], cb5[4], cb6[4], cb7[4];
  const int cb = bid & 63, ks = bid >> 6;
  const int qrow = ks * 144 + w * 18 + (lane >> 3);
  u32x4 wqr[3];
  {
    const int wqv = (lane >> 3) * QN * 2 + (lane & 7) * 16;
    const int wqs = ((ks * 144 + w * 18) * QN + cb * 64) * 2;
    wqr[0] = __builtin_amdgcn_raw_buffer_load_b128(rwq, wqv, wqs, AUX_NT);
    wqr[1] = __builtin_amdgcn_raw_buffer_load_b128(rwq, wqv, wqs + 8 * QN * 2, AUX_NT);
    wqr[2] = __builtin_amdgcn_raw_buffer_load_b128(rwq, lane < 16 ? wqv : 0x7fffffff, wqs + 16 * QN * 2, AUX_NT);
  }
  SB();
  asm volatile("" ::: "memory");
  __builtin_amdgcn_s_barrier();
  asm volatile("" ::: "memory");
  SB();
#pragma unroll
  for (int t = 0; t < 4; ++t) { cb0[t] = LDC(rk, 0, t); SB(); }
#pragma unroll
  for (int t = 0; t < 4; ++t) { cb1[t] = LDC(rk, 1, t); SB(); }
  asm volatile("" :: "v"(xr[0]), "v"(xr[1]), "v"(xr[2]), "v"(xr[3]), "v"(xr[4]), "v"(xr[5]), "v"(xr[6]), "v"(xr[7]), "v"(xr[8]), "v"(b2r));
  SB();

  if (w <= 4) {
    float xv[9];
    float s = 0.f;
#pragma unroll
    for (int j = 0; j < 9; ++j) { xv[j] = bf2f(xr[j]); s += xv[j]; }
    if (w == 4) {
#pragma unroll
      for (int j = 0; j < 9; ++j) ldf[L_X + j * 64 + lane] = xv[j];
      ldf[L_FLAG + 2] = bf2f(b2r);
    }
    const float mean = wave_sum(s) * (1.0f / DM);
    float v = 0.f;
#pragma unroll
    for (int j = 0; j < 9; ++j) { xv[j] -= mean; v = fmaf(xv[j], xv[j], v); }
    const float rstd = 1.0f / sqrtf(wave_sum(v) * (1.0f / DM) + EPS);
    if (w < 4) {
#pragma unroll
      for (int j = 0; j < 9; ++j) lds[L_XN + (j * 64 + lane) * 4 + w] = cvtpk(xv[j] * rstd, 0.f) & 0xffffu;
    } else {
      float s2 = 0.f;
#pragma unroll
      for (int j = 0; j < 9; ++j) { xv[j] = bf_lo(cvtpk(xv[j] * rstd, 0.f)); s2 += xv[j]; }
      const float mean2 = wave_sum(s2) * (1.0f / DM);
      float v2 = 0.f;
#pragma unroll
      for (int j = 0; j < 9; ++j) { xv[j] -= mean2; v2 = fmaf(xv[j], xv[j], v2); }
      const float rstd2 = 1.0f / sqrtf(wave_sum(v2) * (1.0f / DM) + EPS);
#pragma unroll
      for (int j = 0; j < 9; ++j) ldf[L_H + j * 64 + lane] = xv[j] * rstd2;
    }
  } else if (w == 5) {
    if (lane < 2) lds[L_FLAG + lane] = 0u;
  }
  LDS_BARRIER();
  {
    float qa[32];
#pragma unroll
    for (int k = 0; k < 32; ++k) qa[k] = 0.f;
#pragma unroll
    for (int j = 0; j < 3; ++j) {
      const int row = (j < 2) ? qrow + 8 * j : min(qrow + 16, DM - 1);
      const u32x4 xq = *reinterpret_cast<const u32x4*>(lds + L_XN + row * 4);
#pragma unroll
      for (int bb = 0; bb < 4; ++bb) {
        const unsigned xl = xq[bb], xh = xq[bb] << 16;
#pragma unroll
        for (int k = 0; k < 4; ++k) {
          qa[bb * 8 + 2 * k] = dot2(wqr[j][k], xl, qa[bb * 8 + 2 * k]);
          qa[bb * 8 + 2 * k + 1] = dot2(wqr[j][k], xh, qa[bb * 8 + 2 * k + 1]);
        }
      }
    }
#pragma unroll
    for (int k = 0; k < 16; ++k) qa[k] = swap32_add(qa[k], qa[k + 16]);
#pragma unroll
    for (int k = 0; k < 8; ++k) qa[k] = swap16_add(qa[k], qa[k + 8]);
    f32x4 qs;
#pragma unroll
    for (int k = 0; k < 4; ++k) {
      const float t1 = qa[k] + dpp<ROR8>(qa[k]), t2 = qa[k + 4] + dpp<ROR8>(qa[k + 4]);
      qs[k] = (lane & 8) ? t2 : t1;
    }
    *reinterpret_cast<f32x4*>(ldf + L_QP + w * 256 + (lane >> 4) * 64 + (lane & 7) * 8 + ((lane >> 3) & 1) * 4) = qs;
  }
  LDS_BARRIER();
  if (w < 2) {
    float q0 = 0.f, q1 = 0.f;
#pragma unroll
    for (int ww = 0; ww < NWAVE; ++ww) {
      const float2 t = *reinterpret_cast<const float2*>(ldf + L_QP + ww * 256 + tid * 2);
      q0 += t.x; q1 += t.y;
    }
    const u32x4 gq = {tag, __float_as_uint(q0), tag, __float_as_uint(q1)};
    __builtin_amdgcn_raw_buffer_store_b128(gq, rs, WS_QG + (((tid >> 5) * 4 + ks) * QN + cb * 64 + (tid & 31) * 2) * 8, 0, AUX_SC1);
    const int qo = WS_QG + ((b * 4) * QN + kvh * 256 + tid * 2) * 8;
    u32x4 gv[4];
    bool ok = false;
#pragma unroll 1
    for (unsigned spins = 0; spins < SPIN_CAP; ++spins) {
      if (!ok) {
#pragma unroll
        for (int s = 0; s < 4; ++s) gv[s] = __builtin_amdgcn_raw_buffer_load_b128(rs, qo, s * QN * 8, AUX_SC1);
        ok = true;
#pragma unroll
        for (int s = 0; s < 4; ++s) ok &= (gv[s][0] == tag) & (gv[s][2] == tag);
      }
      if (__all(ok)) break;
      asm volatile("" ::: "memory");
    }
    q0 = (__uint_as_float(gv[0][1]) + __uint_as_float(gv[1][1])) + (__uint_as_float(gv[2][1]) + __uint_as_float(gv[3][1]));
    q1 = (__uint_as_float(gv[0][3]) + __uint_as_float(gv[1][3])) + (__uint_as_float(gv[2][3]) + __uint_as_float(gv[3][3]));
    lds[L_QB + tid] = cvtpk(q0, q1);
    asm volatile("" ::: "memory");
    if (lane == 0) __hip_atomic_store(lds + L_FLAG + w, 1u, __ATOMIC_RELAXED, __HIP_MEMORY_SCOPE_WORKGROUP);
    asm volatile("" ::: "memory");
  }
  auto q_ready = [&]() {
    const unsigned long long f = __hip_atomic_load(reinterpret_cast<unsigned long long*>(lds + L_FLAG), __ATOMIC_RELAXED, __HIP_MEMORY_SCOPE_WORKGROUP);
    return __builtin_amdgcn_readfirstlane((unsigned)f & (unsigned)(f >> 32)) != 0;
  };
  bool ready = false;
#define CHUNK(dst, n) do { SB(); _Pragma("unroll") for (int t = 0; t < 4; ++t) dst[t] = LDC(rk, n, t); SB(); \
    if (!ready) { asm volatile("s_waitcnt vmcnt(4)" ::: "memory"); ready = q_ready(); } SB(); } while (0)
  CHUNK(cb2, 2);
  CHUNK(cb3, 3);
  CHUNK(cb4, 4);
  CHUNK(cb5, 5);
  CHUNK(cb6, 6);
#pragma unroll
  for (int t = 0; t < 4; ++t) cb7[t] = LDC(rk, 7, t);
  SB();
  u32x4 mw[4], wt[3];
#pragma unroll
  for (int j = 0; j < 3; ++j) {
    const unsigned short* src = j == 0 ? w1 : (j == 1 ? b1 : w2);
    mw[j] = reinterpret_cast<const u32x4*>(src)[72 * w + lane];
  }
  {
    const int a = lane >> 3;
    const unsigned short* src = a == 0 ? w1 : (a == 1 ? b1 : w2);
    mw[3] = reinterpret_cast<const u32x4*>(src)[72 * w + 64 + (lane & 7)];
  }
#pragma unroll
  for (int j = 0; j < 3; ++j) {
    const int ch = tid + NTHR * j;
    const int row = (ch * 7282) >> 16;
    const int vo = row * (DM * 2) + (ch - row * 9) * 16;
    wt[j] = __builtin_amdgcn_raw_buffer_load_b128(rwo, (j < 2 || tid < 128) ? vo : 0x7fffffff, (h1 * DH * DM + cbk * CB) * 2, AUX_NT);
  }
  SB();
  while (!ready) { __builtin_amdgcn_s_sleep(1); ready = q_ready(); }
  asm volatile("" ::: "memory");
  const u32x4 qh0 = *reinterpret_cast<const u32x4*>(lds + L_QB + c * 4);
  const u32x4 qh1 = *reinterpret_cast<const u32x4*>(lds + L_QB + 64 + c * 4);
  float hv[NOUT];
  float ya[16];
#pragma unroll
  for (int j = 0; j < 16; ++j) ya[j] = 0.f;
#pragma unroll
  for (int j = 0; j < NOUT; ++j) hv[j] = __builtin_bit_cast(float, __builtin_amdgcn_readfirstlane(lds[L_H + col0 + j]));
  const unsigned short* mwl = reinterpret_cast<const unsigned short*>(lds + L_MW + w * MW_W);

  auto mlp_slice = [&](int k) {
    const int f = k * 64 + lane;
    const float w1f = bf2f(mwl[f]), b1f = bf2f(mwl[576 + f]), w2f = bf2f(mwl[2 * 576 + f]);
    constexpr float C1 = -2.0f * 0.7978845608028654f * LOG2E, C2 = C1 * 0.044715f;
#pragma unroll
    for (int j = 0; j < NOUT; ++j) {
      const float uu = fmaf(hv[j], w1f, b1f);
      const float e = ex2(uu * fmaf(uu * uu, C2, C1));
      ya[j] = fmaf(uu * w2f, rcp(1.0f + e), ya[j]);
    }
  };

#define PINY() asm volatile("" : "+v"(ya[0]), "+v"(ya[1]), "+v"(ya[2]), "+v"(ya[3]), "+v"(ya[4]), "+v"(ya[5]), "+v"(ya[6]), "+v"(ya[7]), "+v"(ya[8]))
#define MLP(k) do { mlp_slice(k); PINY(); SB(); } while (0)
#define ISSUE(dst, rs_, so) do { _Pragma("unroll") for (int t = 0; t < 4; ++t) { \
      dst[t] = __builtin_amdgcn_raw_buffer_load_b128(rs_, lo16 + t * 1024, so, AUX_KV); SB(); } } while (0)
#define NOISSUE(dst, rs_, so) do {} while (0)
#define COFF(n) (wpo + ((n) >> 1) * PSTR + ((n) & 1) * 4096)
#define QK_HALF(kb, hf) _Pragma("unroll") for (int t = 0; t < 4; ++t) { \
      float d0 = 0.f, d1 = 0.f; \
      _Pragma("unroll") for (int k = 0; k < 4; ++k) { d0 = dot2(kb[t][k], qh0[k], d0); d1 = dot2(kb[t][k], qh1[k], d1); } \
      xs[8 * (hf) + 2 * t] = d0; xs[8 * (hf) + 2 * t + 1] = d1; }
#define TR_STEP(HALF, CTRL, BIT) _Pragma("unroll") for (int h = 0; h < (HALF); ++h) { \
      const float t1_ = xs[h] + dpp<CTRL>(xs[h]), t2_ = xs[h + (HALF)] + dpp<CTRL>(xs[h + (HALF)]); \
      xs[h] = (c & (BIT)) ? t2_ : t1_; }
#define K_PAGE(pg, blo, bhi, ISS, nlo, nhi, rs_, so) do { \
    float xs[16]; \
    QK_HALF(blo, 0) \
    PIN8(xs); SB(); \
    ISS(nlo, rs_, so); \
    QK_HALF(bhi, 1) \
    PIN8((xs + 8)); SB(); \
    ISS(nhi, rs_, (so) + 4096); \
    TR_STEP(8, ROR8, 8) TR_STEP(4, XOR7, 4) TR_STEP(2, XOR2, 2) TR_STEP(1, XOR1, 1) \
    sc[pg] = xs[0]; \
    asm volatile("" : "+v"(sc[pg])); SB(); \
  } while (0)
#define PV_STEP(vb, t, hf, p) { const float p0 = dpp<BCAST + 8 * (hf) + 2 * (t)>(p), p1 = dpp<BCAST + 8 * (hf) + 2 * (t) + 1>(p); \
      _Pragma("unroll") for (int k = 0; k < 4; ++k) { const float lo = bf_lo(vb[t][k]), hi = bf_hi(vb[t][k]); \
        a0[2 * k] = fmaf(p0, lo, a0[2 * k]); a0[2 * k + 1] = fmaf(p0, hi, a0[2 * k + 1]); \
        a1[2 * k] = fmaf(p1, lo, a1[2 * k]); a1[2 * k + 1] = fmaf(p1, hi, a1[2 * k + 1]); } }
#define V_PAGE(p, blo, bhi, nlo, nhi, so) do { \
    PV_STEP(blo, 0, 0, p) PV_STEP(blo, 1, 0, p) PV_STEP(blo, 2, 0, p) PV_STEP(blo, 3, 0, p) \
    PIN8(a0); PIN8(a1); SB(); \
    ISSUE(nlo, rv, so); \
    PV_STEP(bhi, 0, 1, p) PV_STEP(bhi, 1, 1, p) PV_STEP(bhi, 2, 1, p) PV_STEP(bhi, 3, 1, p) \
    PIN8(a0); PIN8(a1); SB(); \
    ISSUE(nhi, rv, (so) + 4096); \
  } while (0)
  float sc[8];
  K_PAGE(0, cb0, cb1, NOISSUE, cb2, cb3, rk, 0);
  K_PAGE(1, cb2, cb3, NOISSUE, cb4, cb5, rk, 0);
  K_PAGE(2, cb4, cb5, NOISSUE, cb6, cb7, rk, 0);
  K_PAGE(3, cb6, cb7, ISSUE, cb0, cb1, rk, COFF(8));
  {
    unsigned* mwd = lds + L_MW + w * MW_W;
#pragma unroll
    for (int j = 0; j < 3; ++j) *reinterpret_cast<u32x4*>(mwd + j * 288 + lane * 4) = mw[j];
    if (lane < 24) *reinterpret_cast<u32x4*>(mwd + (lane >> 3) * 288 + 256 + (lane & 7) * 4) = mw[3];
#pragma unroll
    for (int j = 0; j < 2; ++j) *reinterpret_cast<u32x4*>(lds + L_WT + (tid + NTHR * j) * 4) = wt[j];
    if (tid < 128) *reinterpret_cast<u32x4*>(lds + L_WT + (tid + NTHR * 2) * 4) = wt[2];
    asm volatile("" ::: "memory");
  }
  MLP(0);
  K_PAGE(4, cb0, cb1, ISSUE, cb2, cb3, rk, COFF(10));
  MLP(1);
  K_PAGE(5, cb2, cb3, ISSUE, cb0, cb1, rk, COFF(12));
  MLP(2);
  K_PAGE(6, cb0, cb1, ISSUE, cb2, cb3, rk, COFF(14));
  MLP(3);
  K_PAGE(7, cb2, cb3, ISSUE, cb0, cb1, rv, COFF(0));
  float m = fmaxf(fmaxf(fmaxf(sc[0], sc[1]), fmaxf(sc[2], sc[3])), fmaxf(fmaxf(sc[4], sc[5]), fmaxf(sc[6], sc[7])));
  m = fmaxf(m, dpp<XOR2>(m));
  m = fmaxf(m, dpp<ROR4>(m));
  m = fmaxf(m, dpp<ROR8>(m));
  m *= SC2;
  float l = 0.f;
#pragma unroll
  for (int k = 0; k < 8; ++k) { sc[k] = ex2(fmaf(sc[k], SC2, -m)); l += sc[k]; }
  float a0[8], a1[8];
#pragma unroll
  for (int k = 0; k < 8; ++k) { a0[k] = 0.f; a1[k] = 0.f; }
  MLP(4);
#pragma unroll 1
  for (int i = 0; i < NPG; i += 2) {
    const int so = wpo + (i + 1) * PSTR;
    V_PAGE(sc[0], cb0, cb1, cb2, cb3, so);
    MLP(5 + (i >> 1));
    V_PAGE(sc[1], cb2, cb3, cb0, cb1, so + PSTR);
#pragma unroll
    for (int k = 0; k < 6; ++k) sc[k] = sc[k + 2];
  }

  {
    const float mw_ = xrow_max(m);
    const float sc_ = ex2(m - mw_);
    float lr = l * sc_;
    lr += dpp<XOR2>(lr);
    lr += dpp<ROR4>(lr);
    lr += dpp<ROR8>(lr);
    lr = xrow_sum(lr);
    const float s0 = dpp<QP0>(sc_), s1 = dpp<QP1>(sc_);
    float t8[8];
#pragma unroll
    for (int k = 0; k < 8; ++k) t8[k] = swap32_add(a0[k] * s0, a1[k] * s1);
    f32x4 t4;
#pragma unroll
    for (int k = 0; k < 4; ++k) t4[k] = swap16_add(t8[k], t8[k + 4]);
    float* rec = ldf + L_MW + w * MW_W;
    *reinterpret_cast<f32x4*>(rec + (lane >> 5) * 128 + c * 8 + (r & 1) * 4) = t4;
    if (lane < 2) { rec[256 + lane * 2] = mw_; rec[257 + lane * 2] = lr; }
  }
  LDS_BARRIER();
  if (w < 2) {
    float mm[NWAVE], ll[NWAVE];
    float M = -INFINITY;
#pragma unroll
    for (int ww = 0; ww < NWAVE; ++ww) {
      const float2 t = *reinterpret_cast<const float2*>(ldf + L_MW + ww * MW_W + 256 + w * 2);
      mm[ww] = t.x; ll[ww] = t.y; M = fmaxf(M, t.x);
    }
    float O0 = 0.f, O1 = 0.f, L = 0.f;
#pragma unroll
    for (int ww = 0; ww < NWAVE; ++ww) {
      const float wgt = ex2(mm[ww] - M);
      L = fmaf(wgt, ll[ww], L);
      const float2 t = *reinterpret_cast<const float2*>(ldf + L_MW + ww * MW_W + w * 128 + lane * 2);
      O0 = fmaf(wgt, t.x, O0); O1 = fmaf(wgt, t.y, O1);
    }
    const int ro = WS_OG + (prod * 2 + w) * OREC;
    const u32x4 go = {tag, __float_as_uint(O0), tag, __float_as_uint(O1)};
    __builtin_amdgcn_raw_buffer_store_b128(go, rs, lo16, ro, AUX_SC1);
    const u32x4 gm = {tag, __float_as_uint(M), tag, __float_as_uint(L)};
    __builtin_amdgcn_raw_buffer_store_b128(gm, rs, lane == 0 ? 1024 : 0x7fffffff, ro, AUX_SC1);
  }
  {
    const int ro = WS_OG + (((w >> 1) * 64 + (h1 >> 1) * 4) * 2 + (h1 & 1)) * OREC;
    const int vo = r * 2 * OREC + ((w & 1) * 64 + c * 4) * 8;
    u32x4 g0, g1, gm;
    g0 = __builtin_amdgcn_raw_buffer_load_b128(rs, vo, ro, AUX_SC1);
    g1 = __builtin_amdgcn_raw_buffer_load_b128(rs, vo + 16, ro, AUX_SC1);
    gm = __builtin_amdgcn_raw_buffer_load_b128(rs, r * 2 * OREC + 1024, ro, AUX_SC1);
    SB();
    const float ysum = wave_treduce16(ya, lane);
    if ((lane & 3) == 0) ldf[L_Y + w * 16 + (lane >> 2)] = ysum;
    u32x4 bfr[4];
    if (w < 5) {
      const unsigned short* wts = reinterpret_cast<const unsigned short*>(lds + L_WT) + (r * 8) * CB + w * 16 + c;
#pragma unroll
      for (int kk = 0; kk < 4; ++kk)
#pragma unroll
        for (int j = 0; j < 4; ++j)
          bfr[kk][j] = (unsigned)wts[(kk * 32 + 2 * j) * CB] | ((unsigned)wts[(kk * 32 + 2 * j + 1) * CB] << 16);
    }
    SB();
    bool ok = (g0[0] == tag) & (g0[2] == tag) & (g1[0] == tag) & (g1[2] == tag) & (gm[0] == tag) & (gm[2] == tag);
#pragma unroll 1
    for (unsigned spins = 0; spins < SPIN_CAP; ++spins) {
      const unsigned long long miss = __ballot(!ok);
      if (miss == 0) break;
      if (__popcll(miss) > 16) __builtin_amdgcn_s_sleep(16);
      if (!ok) {
        g0 = __builtin_amdgcn_raw_buffer_load_b128(rs, vo, ro, AUX_SC1);
        g1 = __builtin_amdgcn_raw_buffer_load_b128(rs, vo + 16, ro, AUX_SC1);
        gm = __builtin_amdgcn_raw_buffer_load_b128(rs, r * 2 * OREC + 1024, ro, AUX_SC1);
        ok = (g0[0] == tag) & (g0[2] == tag) & (g1[0] == tag) & (g1[2] == tag) & (gm[0] == tag) & (gm[2] == tag);
      }
      asm volatile("" ::: "memory");
    }
    const float Ms = __uint_as_float(gm[1]);
    const float wgt = ex2(Ms - xrow_max(Ms));
    const float Ls = xrow_sum(wgt * __uint_as_float(gm[3]));
    const float t0 = swap32_add(wgt * __uint_as_float(g0[1]), wgt * __uint_as_float(g1[1]));
    const float t1 = swap32_add(wgt * __uint_as_float(g0[3]), wgt * __uint_as_float(g1[3]));
    const float ov = swap16_add(t0, t1) * rcp(Ls);
    reinterpret_cast<unsigned short*>(lds + L_O)[(w >> 1) * DH + (w & 1) * 64 + c * 4 + r] = (unsigned short)(cvtpk(ov, 0.f) & 0xffffu);
    LDS_BARRIER();
    if (w < 5) {
      f32x4 acc0 = {0.f, 0.f, 0.f, 0.f}, acc1 = {0.f, 0.f, 0.f, 0.f};
      u32x4 afr[4];
#pragma unroll
      for (int kk = 0; kk < 4; ++kk) afr[kk] = *reinterpret_cast<const u32x4*>(lds + L_O + (c & 3) * (DH / 2) + kk * 16 + r * 4);
      acc0 = __builtin_amdgcn_mfma_f32_16x16x32_bf16(__builtin_bit_cast(bf16x8, bfr[0]), __builtin_bit_cast(bf16x8, afr[0]), acc0, 0, 0, 0);
      acc1 = __builtin_amdgcn_mfma_f32_16x16x32_bf16(__builtin_bit_cast(bf16x8, bfr[1]), __builtin_bit_cast(bf16x8, afr[1]), acc1, 0, 0, 0);
      acc0 = __builtin_amdgcn_mfma_f32_16x16x32_bf16(__builtin_bit_cast(bf16x8, bfr[2]), __builtin_bit_cast(bf16x8, afr[2]), acc0, 0, 0, 0);
      acc1 = __builtin_amdgcn_mfma_f32_16x16x32_bf16(__builtin_bit_cast(bf16x8, bfr[3]), __builtin_bit_cast(bf16x8, afr[3]), acc1, 0, 0, 0);
      const int so = (c < 8 && w * 16 + r * 4 < CB) ? (c & 3) * SROW + r * 32 + (c >> 2) * 16 : 0x7fffffff;
      const int sb = WS_SG + (h1 * 8 + cbk) * SREC + w * 128;
      const float e0 = (c & 4) ? acc0[2] + acc1[2] : acc0[0] + acc1[0], e1 = (c & 4) ? acc0[3] + acc1[3] : acc0[1] + acc1[1];
      const u32x4 s0 = {tag, __float_as_uint(e0), tag, __float_as_uint(e1)};
      __builtin_amdgcn_raw_buffer_store_b128(s0, rs, so, sb, AUX_SC1);
    } else {
      const int odd = prod & 1, pr = 2 * (w - 5) + (r >> 1);
      const int j0 = 2 * pr - odd;
      const bool v0 = j0 >= 0 && j0 < NOUT, v1 = j0 + 1 < NOUT;
      const int so = WS_SG + ((prod & 63) >> 3) * SREC + b * SROW + ((prod & 7) * NOUT - odd) * 8;
      const int vo2 = pr < 5 ? ((r & 1) * 16 + c) * 8 * SREC + pr * 16 : 0x7fffffff;
      u32x4 gs = __builtin_amdgcn_raw_buffer_load_b128(rs, vo2, so, AUX_SC1);
      SB();
      const int jj = min(max(j0 + (c & 1), 0), NOUT - 1);
      float y = ldf[L_FLAG + 2];
#pragma unroll
      for (int ww = 0; ww < NWAVE; ++ww) y += ldf[L_Y + ww * 16 + jj];
      const float g = y / (1.0f + __expf(-y));
      const float xo = ldf[L_X + col0 + jj];
      SB();
      bool ok2 = (pr >= 5) | ((gs[0] == tag) & (gs[2] == tag));
#pragma unroll 1
      for (unsigned spins = 0; spins < SPIN_CAP; ++spins) {
        if (__all(ok2)) break;
        if (!ok2) {
          gs = __builtin_amdgcn_raw_buffer_load_b128(rs, vo2, so, AUX_SC1);
          ok2 = (gs[0] == tag) & (gs[2] == tag);
        }
        asm volatile("" ::: "memory");
      }
      const float e0 = __uint_as_float(gs[1]), e1 = __uint_as_float(gs[3]);
      const float u0 = e0 + dpp<XOR1>(e0), u1 = e1 + dpp<XOR1>(e1);
      float s = (c & 1) ? u1 : u0;
      s += dpp<XOR2>(s);
      s += dpp<ROR4>(s);
      s += dpp<ROR8>(s);
      s = swap16_add(s, s);
      if ((r & 1) == 0 && c < 2 && (c ? v1 : v0)) out[b * DM + col0 + j0 + c] = (unsigned short)(cvtpk(fmaf(s, g, xo), 0.f) & 0xffffu);
      if (bid == 0 && w == 5 && lane == 0) __hip_atomic_store(reinterpret_cast<unsigned*>(ws + WS_EPOCH), tag, RLX_AGENT);
    }
  }
}

}

extern "C" void kernel_launch(void* const* d_in, const int* in_sizes, int n_in, void* d_out, int out_size, void* d_ws, size_t ws_size,
                              hipStream_t stream) {
  (void)in_sizes; (void)n_in; (void)out_size; (void)ws_size;
  fused_decode_kernel<<<NWG, NTHR, 0, stream>>>(
      (const unsigned short*)d_in[0], (const unsigned short*)d_in[1], (const unsigned short*)d_in[2], (const unsigned short*)d_in[3],
      (const unsigned short*)d_in[4], (const unsigned short*)d_in[6], (const unsigned short*)d_in[7], (const unsigned short*)d_in[8],
      (const unsigned short*)d_in[9], (unsigned short*)d_out, (unsigned char*)d_ws);
}
```

```cpp
#include <hip/hip_runtime.h>
#include <hip/hip_bf16.h>
#include <stdint.h>

#define ONEST 1
#define OUTAUX -1
namespace {

constexpr int NB = 4, DM = 576, HQ = 32, HKV = 16, SK = 8192, DH = 128, FF = 4608;
constexpr int NWG = 256, NTHR = 512, NWAVE = 8;
constexpr int NSPLIT = 4;
constexpr int KEYS_WG = SK / NSPLIT;
constexpr int PAGE = 32;
constexpr int NPG = KEYS_WG / PAGE / NWAVE;
constexpr int QN = HQ * DH;
constexpr float LOG2E = 1.4426950408889634f;
constexpr float SC2 = 0.08838834764831845f * LOG2E;
constexpr float EPS = 1e-5f;
constexpr int DEPTH = 2;
constexpr int CB = 72;
constexpr int NOUT = 9;

constexpr int WS_EPOCH = 0;
constexpr int WS_QG = 4096;
constexpr int WS_OG = WS_QG + NB * 4 * QN * 8;
constexpr int OREC = 1152;
constexpr int WS_SG = WS_OG + NWG * 2 * OREC;
constexpr int SROW = 640, SREC = NB * SROW;
constexpr int WS_BYTES = WS_SG + HQ * 8 * SREC;
constexpr unsigned TAG_STEP = 0x9E3779B1u;
constexpr unsigned SPIN_CAP = 1u << 20;

constexpr int L_XN = 0;
constexpr int L_QP = L_XN + DM * 4;
constexpr int L_QB = L_QP + NWAVE * 256;
constexpr int L_FLAG = L_QB + 128;
constexpr int L_H = L_FLAG + 4;
constexpr int L_X = L_H + DM;
constexpr int L_MW = L_X + DM;
constexpr int MW_W = 3 * 288;
constexpr int L_WT = L_MW + NWAVE * MW_W;
constexpr int L_O = L_WT + DH * CB / 2 + 8;
constexpr int L_Y = L_O + NB * DH / 2;
constexpr int L_WORDS = L_Y + NWAVE * 16;
static_assert(L_WORDS * 4 <= 163840, "LDS");
static_assert((L_QP % 4 | L_QB % 4 | L_MW % 4 | L_WT % 4 | L_O % 4 | L_FLAG % 2) == 0, "align");

typedef unsigned u32x4 __attribute__((ext_vector_type(4)));
typedef unsigned u32x2 __attribute__((ext_vector_type(2)));
typedef float f32x4 __attribute__((ext_vector_type(4)));
typedef __bf16 bf16x2 __attribute__((ext_vector_type(2)));
typedef __bf16 bf16x8 __attribute__((ext_vector_type(8)));
#define RLX_AGENT __ATOMIC_RELAXED, __HIP_MEMORY_SCOPE_AGENT
constexpr int AUX_SC1 = 16, AUX_NT = 2, AUX_KV = 18;

__device__ __forceinline__ float dot2(unsigned a, unsigned b, float acc) {
  return __builtin_amdgcn_fdot2_f32_bf16(__builtin_bit_cast(bf16x2, a), __builtin_bit_cast(bf16x2, b), acc, false);
}
__device__ __forceinline__ unsigned cvtpk(float lo, float hi) {
  bf16x2 r = {(__bf16)lo, (__bf16)hi};
  return __builtin_bit_cast(unsigned, r);
}
__device__ __forceinline__ float bf_lo(unsigned v) { return __builtin_bit_cast(float, v << 16); }
__device__ __forceinline__ float bf_hi(unsigned v) { return __builtin_bit_cast(float, v & 0xffff0000u); }
__device__ __forceinline__ float bf2f(unsigned short v) { return __builtin_bit_cast(float, (unsigned)v << 16); }
template <int CTRL> __device__ __forceinline__ float dpp(float x) {
  return __builtin_bit_cast(float, __builtin_amdgcn_mov_dpp(__builtin_bit_cast(int, x), CTRL, 0xf, 0xf, true));
}
constexpr int XOR1 = 0xB1, XOR2 = 0x4E, XOR7 = 0x141, ROR8 = 0x128, ROR4 = 0x124, BCAST = 0x150;
constexpr int QP0 = 0x00, QP1 = 0x55;
__device__ __forceinline__ float ex2(float x) { return __builtin_amdgcn_exp2f(x); }
__device__ __forceinline__ float rcp(float x) { return __builtin_amdgcn_rcpf(x); }

__device__ __forceinline__ float swap32_add(float a, float b) {
  auto s = __builtin_amdgcn_permlane32_swap(__float_as_uint(a), __float_as_uint(b), false, false);
  return __uint_as_float(s[0]) + __uint_as_float(s[1]);
}
__device__ __forceinline__ float swap16_add(float a, float b) {
  auto s = __builtin_amdgcn_permlane16_swap(__float_as_uint(a), __float_as_uint(b), false, false);
  return __uint_as_float(s[0]) + __uint_as_float(s[1]);
}
__device__ __forceinline__ float xrow_max(float x) {
  auto s = __builtin_amdgcn_permlane16_swap(__float_as_uint(x), __float_as_uint(x), false, false);
  x = fmaxf(__uint_as_float(s[0]), __uint_as_float(s[1]));
  auto t = __builtin_amdgcn_permlane32_swap(__float_as_uint(x), __float_as_uint(x), false, false);
  return fmaxf(__uint_as_float(t[0]), __uint_as_float(t[1]));
}
__device__ __forceinline__ float xrow_sum(float x) {
  x = swap16_add(x, x);
  return swap32_add(x, x);
}
__device__ __forceinline__ float row_sum(float x) {
  x += dpp<XOR1>(x);
  x += dpp<XOR2>(x);
  x += dpp<XOR7>(x);
  x += dpp<ROR8>(x);
  return x;
}
__device__ __forceinline__ float wave_sum(float x) { return xrow_sum(row_sum(x)); }
__device__ __forceinline__ float wave_treduce16(float (&v)[16], int lane) {
#pragma unroll
  for (int j = 0; j < 8; ++j) v[j] = swap32_add(v[j], v[j + 8]);
#pragma unroll
  for (int j = 0; j < 4; ++j) v[j] = swap16_add(v[j], v[j + 4]);
#pragma unroll
  for (int j = 0; j < 2; ++j) {
    const float a = v[j] + dpp<ROR8>(v[j]), b = v[j + 2] + dpp<ROR8>(v[j + 2]);
    v[j] = (lane & 8) ? b : a;
  }
  {
    const float a = v[0] + dpp<XOR7>(v[0]), b = v[1] + dpp<XOR7>(v[1]);
    v[0] = (lane & 4) ? b : a;
  }
  float r = v[0];
  r += dpp<XOR2>(r);
  r += dpp<XOR1>(r);
  return r;
}

#define SB() __builtin_amdgcn_sched_barrier(0)
#define PIN8(a) asm volatile("" : "+v"(a[0]), "+v"(a[1]), "+v"(a[2]), "+v"(a[3]), "+v"(a[4]), "+v"(a[5]), "+v"(a[6]), "+v"(a[7]) : : "memory")
#define LDS_BARRIER() do { asm volatile("s_waitcnt lgkmcnt(0)" ::: "memory"); __builtin_amdgcn_s_barrier(); asm volatile("" ::: "memory"); } while (0)

__global__ void __launch_bounds__(NTHR, 2)
fused_decode_kernel(const unsigned short* __restrict__ x, const unsigned short* __restrict__ kc, const unsigned short* __restrict__ vc,
                    const unsigned short* __restrict__ wq, const unsigned short* __restrict__ wo, const unsigned short* __restrict__ w1,
                    const unsigned short* __restrict__ b1, const unsigned short* __restrict__ w2, const unsigned short* __restrict__ b2,
                    unsigned short* __restrict__ out, unsigned char* ws) {
  __shared__ __attribute__((aligned(16))) unsigned lds[L_WORDS];
  float* ldf = reinterpret_cast<float*>(lds);
  const int tid = threadIdx.x, lane = tid & 63, w = __builtin_amdgcn_readfirstlane(tid >> 6);
  const int r = lane >> 4, c = lane & 15;
  const int bid = blockIdx.x, xcd = bid & 7, ii = bid >> 3;
  const int kvh = ii & 15, b = (xcd >> 1) & 3, sp = ((xcd & 1) << 1) | (ii >> 4);
  const int prod = b * 64 + kvh * 4 + sp;
  const int col0 = (prod & 63) * NOUT;
  const int h1 = 4 * xcd + (ii >> 3), cbk = ii & 7;

  const unsigned tag = *reinterpret_cast<const __attribute__((address_space(4))) unsigned*>(reinterpret_cast<uintptr_t>(ws + WS_EPOCH)) + TAG_STEP;
  const int lnb = w < 4 ? w : b;
  const __amdgpu_buffer_rsrc_t rx = __builtin_amdgcn_make_buffer_rsrc((void*)x, 0, NB * DM * 2, 0x00020000);
  const __amdgpu_buffer_rsrc_t rb2 = __builtin_amdgcn_make_buffer_rsrc((void*)b2, 0, 2, 0x00020000);
  unsigned short xr[9];
#pragma unroll
  for (int j = 0; j < 9; ++j) xr[j] = __builtin_amdgcn_raw_buffer_load_b16(rx, lane * 2 + j * 128, w <= 4 ? lnb * DM * 2 : 0x10000, 0);
  const unsigned short b2r = __builtin_amdgcn_raw_buffer_load_b16(rb2, 0, w == 4 ? 0 : 0x10000, 0);
  const size_t kvbase = ((size_t)(b * HKV + kvh) * SK + (size_t)sp * KEYS_WG) * DH;
  const __amdgpu_buffer_rsrc_t rk = __builtin_amdgcn_make_buffer_rsrc((void*)(kc + kvbase), 0, KEYS_WG * DH * 2, 0x00020000);
  const __amdgpu_buffer_rsrc_t rv = __builtin_amdgcn_make_buffer_rsrc((void*)(vc + kvbase), 0, KEYS_WG * DH * 2, 0x00020000);
  const __amdgpu_buffer_rsrc_t rwq = __builtin_amdgcn_make_buffer_rsrc((void*)wq, 0, DM * QN * 2, 0x00020000);
  const __amdgpu_buffer_rsrc_t rwo = __builtin_amdgcn_make_buffer_rsrc((void*)wo, 0, DM * QN * 2, 0x00020000);
  const __amdgpu_buffer_rsrc_t rs = __builtin_amdgcn_make_buffer_rsrc((void*)ws, 0, WS_BYTES, 0x00020000);
  constexpr int PBYTES = PAGE * DH * 2;
  constexpr int PSTR = NWAVE * PBYTES;
  const int lo16 = lane * 16;
  const int wpo = w * PBYTES;
#define LDC(rs_, n, t) __builtin_amdgcn_raw_buffer_load_b128(rs_, lo16 + (t) * 1024, wpo + ((n) >> 1) * PSTR + ((n) & 1) * 4096, AUX_KV)
  u32x4 cb0[4], cb1[4], cb2[4], cb3[4], cb4[4], cb5[4], cb6[4], cb7[4];
  const int cb = bid & 63, ks = bid >> 6;
  const int qrow = ks * 144 + w * 18 + (lane >> 3);
  u32x4 wqr[3];
  {
    const int wqv = (lane >> 3) * QN * 2 + (lane & 7) * 16;
    const int wqs = ((ks * 144 + w * 18) * QN + cb * 64) * 2;
    wqr[0] = __builtin_amdgcn_raw_buffer_load_b128(rwq, wqv, wqs, AUX_NT);
    wqr[1] = __builtin_amdgcn_raw_buffer_load_b128(rwq, wqv, wqs + 8 * QN * 2, AUX_NT);
    wqr[2] = __builtin_amdgcn_raw_buffer_load_b128(rwq, lane < 16 ? wqv : 0x7fffffff, wqs + 16 * QN * 2, AUX_NT);
  }
  SB();
  asm volatile("" ::: "memory");
  __builtin_amdgcn_s_barrier();
  asm volatile("" ::: "memory");
  SB();
#pragma unroll
  for (int t = 0; t < 4; ++t) { cb0[t] = LDC(rk, 0, t); SB(); }
#pragma unroll
  for (int t = 0; t < 4; ++t) { cb1[t] = LDC(rk, 1, t); SB(); }
  asm volatile("" :: "v"(xr[0]), "v"(xr[1]), "v"(xr[2]), "v"(xr[3]), "v"(xr[4]), "v"(xr[5]), "v"(xr[6]), "v"(xr[7]), "v"(xr[8]), "v"(b2r));
  SB();

  if (w <= 4) {
    float xv[9];
    float s = 0.f;
#pragma unroll
    for (int j = 0; j < 9; ++j) { xv[j] = bf2f(xr[j]); s += xv[j]; }
    if (w == 4) {
#pragma unroll
      for (int j = 0; j < 9; ++j) ldf[L_X + j * 64 + lane] = xv[j];
      ldf[L_FLAG + 2] = bf2f(b2r);
    }
    const float mean = wave_sum(s) * (1.0f / DM);
    float v = 0.f;
#pragma unroll
    for (int j = 0; j < 9; ++j) { xv[j] -= mean; v = fmaf(xv[j], xv[j], v); }
    const float rstd = 1.0f / sqrtf(wave_sum(v) * (1.0f / DM) + EPS);
    if (w < 4) {
#pragma unroll
      for (int j = 0; j < 9; ++j) lds[L_XN + (j * 64 + lane) * 4 + w] = cvtpk(xv[j] * rstd, 0.f) & 0xffffu;
    } else {
      float s2 = 0.f;
#pragma unroll
      for (int j = 0; j < 9; ++j) { xv[j] = bf_lo(cvtpk(xv[j] * rstd, 0.f)); s2 += xv[j]; }
      const float mean2 = wave_sum(s2) * (1.0f / DM);
      float v2 = 0.f;
#pragma unroll
      for (int j = 0; j < 9; ++j) { xv[j] -= mean2; v2 = fmaf(xv[j], xv[j], v2); }
      const float rstd2 = 1.0f / sqrtf(wave_sum(v2) * (1.0f / DM) + EPS);
#pragma unroll
      for (int j = 0; j < 9; ++j) ldf[L_H + j * 64 + lane] = xv[j] * rstd2;
    }
  } else if (w == 5) {
    if (lane < 2) lds[L_FLAG + lane] = 0u;
  }
  LDS_BARRIER();
  {
    float qa[32];
#pragma unroll
    for (int k = 0; k < 32; ++k) qa[k] = 0.f;
#pragma unroll
    for (int j = 0; j < 3; ++j) {
      const int row = (j < 2) ? qrow + 8 * j : min(qrow + 16, DM - 1);
      const u32x4 xq = *reinterpret_cast<const u32x4*>(lds + L_XN + row * 4);
#pragma unroll
      for (int bb = 0; bb < 4; ++bb) {
        const unsigned xl = xq[bb], xh = xq[bb] << 16;
#pragma unroll
        for (int k = 0; k < 4; ++k) {
          qa[bb * 8 + 2 * k] = dot2(wqr[j][k], xl, qa[bb * 8 + 2 * k]);
          qa[bb * 8 + 2 * k + 1] = dot2(wqr[j][k], xh, qa[bb * 8 + 2 * k + 1]);
        }
      }
    }
#pragma unroll
    for (int k = 0; k < 16; ++k) qa[k] = swap32_add(qa[k], qa[k + 16]);
#pragma unroll
    for (int k = 0; k < 8; ++k) qa[k] = swap16_add(qa[k], qa[k + 8]);
    f32x4 qs;
#pragma unroll
    for (int k = 0; k < 4; ++k) {
      const float t1 = qa[k] + dpp<ROR8>(qa[k]), t2 = qa[k + 4] + dpp<ROR8>(qa[k + 4]);
      qs[k] = (lane & 8) ? t2 : t1;
    }
    *reinterpret_cast<f32x4*>(ldf + L_QP + w * 256 + (lane >> 4) * 64 + (lane & 7) * 8 + ((lane >> 3) & 1) * 4) = qs;
  }
  LDS_BARRIER();
  if (w < 2) {
    float q0 = 0.f, q1 = 0.f;
#pragma unroll
    for (int ww = 0; ww < NWAVE; ++ww) {
      const float2 t = *reinterpret_cast<const float2*>(ldf + L_QP + ww * 256 + tid * 2);
      q0 += t.x; q1 += t.y;
    }
    const u32x4 gq = {tag, __float_as_uint(q0), tag, __float_as_uint(q1)};
    __builtin_amdgcn_raw_buffer_store_b128(gq, rs, WS_QG + (((tid >> 5) * 4 + ks) * QN + cb * 64 + (tid & 31) * 2) * 8, 0, AUX_SC1);
    const int qo = WS_QG + ((b * 4) * QN + kvh * 256 + tid * 2) * 8;
    u32x4 gv[4];
    bool ok = false;
#pragma unroll 1
    for (unsigned spins = 0; spins < SPIN_CAP; ++spins) {
      if (!ok) {
#pragma unroll
        for (int s = 0; s < 4; ++s) gv[s] = __builtin_amdgcn_raw_buffer_load_b128(rs, qo, s * QN * 8, AUX_SC1);
        ok = true;
#pragma unroll
        for (int s = 0; s < 4; ++s) ok &= (gv[s][0] == tag) & (gv[s][2] == tag);
      }
      if (__all(ok)) break;
      asm volatile("" ::: "memory");
    }
    q0 = (__uint_as_float(gv[0][1]) + __uint_as_float(gv[1][1])) + (__uint_as_float(gv[2][1]) + __uint_as_float(gv[3][1]));
    q1 = (__uint_as_float(gv[0][3]) + __uint_as_float(gv[1][3])) + (__uint_as_float(gv[2][3]) + __uint_as_float(gv[3][3]));
    lds[L_QB + tid] = cvtpk(q0, q1);
    asm volatile("" ::: "memory");
    if (lane == 0) __hip_atomic_store(lds + L_FLAG + w, 1u, __ATOMIC_RELAXED, __HIP_MEMORY_SCOPE_WORKGROUP);
    asm volatile("" ::: "memory");
  }
  auto q_ready = [&]() {
    const unsigned long long f = __hip_atomic_load(reinterpret_cast<unsigned long long*>(lds + L_FLAG), __ATOMIC_RELAXED, __HIP_MEMORY_SCOPE_WORKGROUP);
    return __builtin_amdgcn_readfirstlane((unsigned)f & (unsigned)(f >> 32)) != 0;
  };
  bool ready = false;
#define CHUNK(dst, n) do { SB(); _Pragma("unroll") for (int t = 0; t < 4; ++t) dst[t] = LDC(rk, n, t); SB(); \
    if (!ready) { asm volatile("s_waitcnt vmcnt(4)" ::: "memory"); ready = q_ready(); } SB(); } while (0)
  CHUNK(cb2, 2);
  CHUNK(cb3, 3);
  CHUNK(cb4, 4);
  CHUNK(cb5, 5);
  CHUNK(cb6, 6);
#pragma unroll
  for (int t = 0; t < 4; ++t) cb7[t] = LDC(rk, 7, t);
  SB();
  u32x4 mw[4], wt[3];
#pragma unroll
  for (int j = 0; j < 3; ++j) {
    const unsigned short* src = j == 0 ? w1 : (j == 1 ? b1 : w2);
    mw[j] = reinterpret_cast<const u32x4*>(src)[72 * w + lane];
  }
  {
    const int a = lane >> 3;
    const unsigned short* src = a == 0 ? w1 : (a == 1 ? b1 : w2);
    mw[3] = reinterpret_cast<const u32x4*>(src)[72 * w + 64 + (lane & 7)];
  }
#pragma unroll
  for (int j = 0; j < 3; ++j) {
    const int ch = tid + NTHR * j;
    const int row = (ch * 7282) >> 16;
    const int vo = row * (DM * 2) + (ch - row * 9) * 16;
    wt[j] = __builtin_amdgcn_raw_buffer_load_b128(rwo, (j < 2 || tid < 128) ? vo : 0x7fffffff, (h1 * DH * DM + cbk * CB) * 2, AUX_NT);
  }
  SB();
  while (!ready) { __builtin_amdgcn_s_sleep(1); ready = q_ready(); }
  asm volatile("" ::: "memory");
  const u32x4 qh0 = *reinterpret_cast<const u32x4*>(lds + L_QB + c * 4);
  const u32x4 qh1 = *reinterpret_cast<const u32x4*>(lds + L_QB + 64 + c * 4);
  float hv[NOUT];
  float ya[16];
#pragma unroll
  for (int j = 0; j < 16; ++j) ya[j] = 0.f;
#pragma unroll
  for (int j = 0; j < NOUT; ++j) hv[j] = __builtin_bit_cast(float, __builtin_amdgcn_readfirstlane(lds[L_H + col0 + j]));
  const unsigned short* mwl = reinterpret_cast<const unsigned short*>(lds + L_MW + w * MW_W);

  auto mlp_slice = [&](int k) {
    const int f = k * 64 + lane;
    const float w1f = bf2f(mwl[f]), b1f = bf2f(mwl[576 + f]), w2f = bf2f(mwl[2 * 576 + f]);
    constexpr float C1 = -2.0f * 0.7978845608028654f * LOG2E, C2 = C1 * 0.044715f;
#pragma unroll
    for (int j = 0; j < NOUT; ++j) {
      const float uu = fmaf(hv[j], w1f, b1f);
      const float e = ex2(uu * fmaf(uu * uu, C2, C1));
      ya[j] = fmaf(uu * w2f, rcp(1.0f + e), ya[j]);
    }
  };

#define PINY() asm volatile("" : "+v"(ya[0]), "+v"(ya[1]), "+v"(ya[2]), "+v"(ya[3]), "+v"(ya[4]), "+v"(ya[5]), "+v"(ya[6]), "+v"(ya[7]), "+v"(ya[8]))
#define MLP(k) do { mlp_slice(k); PINY(); SB(); } while (0)
#define ISSUE(dst, rs_, so) do { _Pragma("unroll") for (int t = 0; t < 4; ++t) { \
      dst[t] = __builtin_amdgcn_raw_buffer_load_b128(rs_, lo16 + t * 1024, so, AUX_KV); SB(); } } while (0)
#define NOISSUE(dst, rs_, so) do {} while (0)
#define COFF(n) (wpo + ((n) >> 1) * PSTR + ((n) & 1) * 4096)
#define QK_HALF(kb, hf) _Pragma("unroll") for (int t = 0; t < 4; ++t) { \
      float d0 = 0.f, d1 = 0.f; \
      _Pragma("unroll") for (int k = 0; k < 4; ++k) { d0 = dot2(kb[t][k], qh0[k], d0); d1 = dot2(kb[t][k], qh1[k], d1); } \
      xs[8 * (hf) + 2 * t] = d0; xs[8 * (hf) + 2 * t + 1] = d1; }
#define TR_STEP(HALF, CTRL, BIT) _Pragma("unroll") for (int h = 0; h < (HALF); ++h) { \
      const float t1_ = xs[h] + dpp<CTRL>(xs[h]), t2_ = xs[h + (HALF)] + dpp<CTRL>(xs[h + (HALF)]); \
      xs[h] = (c & (BIT)) ? t2_ : t1_; }
#define K_PAGE(pg, blo, bhi, ISS, nlo, nhi, rs_, so) do { \
    float xs[16]; \
    QK_HALF(blo, 0) \
    PIN8(xs); SB(); \
    ISS(nlo, rs_, so); \
    QK_HALF(bhi, 1) \
    PIN8((xs + 8)); SB(); \
    ISS(nhi, rs_, (so) + 4096); \
    TR_STEP(8, ROR8, 8) TR_STEP(4, XOR7, 4) TR_STEP(2, XOR2, 2) TR_STEP(1, XOR1, 1) \
    sc[pg] = xs[0]; \
    asm volatile("" : "+v"(sc[pg])); SB(); \
  } while (0)
#define PV_STEP(vb, t, hf, p) { const float p0 = dpp<BCAST + 8 * (hf) + 2 * (t)>(p), p1 = dpp<BCAST + 8 * (hf) + 2 * (t) + 1>(p); \
      _Pragma("unroll") for (int k = 0; k < 4; ++k) { const float lo = bf_lo(vb[t][k]), hi = bf_hi(vb[t][k]); \
        a0[2 * k] = fmaf(p0, lo, a0[2 * k]); a0[2 * k + 1] = fmaf(p0, hi, a0[2 * k + 1]); \
        a1[2 * k] = fmaf(p1, lo, a1[2 * k]); a1[2 * k + 1] = fmaf(p1, hi, a1[2 * k + 1]); } }
#define V_PAGE(p, blo, bhi, nlo, nhi, so) do { \
    PV_STEP(blo, 0, 0, p) PV_STEP(blo, 1, 0, p) PV_STEP(blo, 2, 0, p) PV_STEP(blo, 3, 0, p) \
    PIN8(a0); PIN8(a1); SB(); \
    ISSUE(nlo, rv, so); \
    PV_STEP(bhi, 0, 1, p) PV_STEP(bhi, 1, 1, p) PV_STEP(bhi, 2, 1, p) PV_STEP(bhi, 3, 1, p) \
    PIN8(a0); PIN8(a1); SB(); \
    ISSUE(nhi, rv, (so) + 4096); \
  } while (0)
  float sc[8];
  K_PAGE(0, cb0, cb1, NOISSUE, cb2, cb3, rk, 0);
  K_PAGE(1, cb2, cb3, NOISSUE, cb4, cb5, rk, 0);
  K_PAGE(2, cb4, cb5, NOISSUE, cb6, cb7, rk, 0);
  K_PAGE(3, cb6, cb7, ISSUE, cb0, cb1, rk, COFF(8));
  {
    unsigned* mwd = lds + L_MW + w * MW_W;
#pragma unroll
    for (int j = 0; j < 3; ++j) *reinterpret_cast<u32x4*>(mwd + j * 288 + lane * 4) = mw[j];
    if (lane < 24) *reinterpret_cast<u32x4*>(mwd + (lane >> 3) * 288 + 256 + (lane & 7) * 4) = mw[3];
#pragma unroll
    for (int j = 0; j < 2; ++j) *reinterpret_cast<u32x4*>(lds + L_WT + (tid + NTHR * j) * 4) = wt[j];
    if (tid < 128) *reinterpret_cast<u32x4*>(lds + L_WT + (tid + NTHR * 2) * 4) = wt[2];
    asm volatile("" ::: "memory");
  }
  MLP(0);
  K_PAGE(4, cb0, cb1, ISSUE, cb2, cb3, rk, COFF(10));
  MLP(1);
  K_PAGE(5, cb2, cb3, ISSUE, cb0, cb1, rk, COFF(12));
  MLP(2);
  K_PAGE(6, cb0, cb1, ISSUE, cb2, cb3, rk, COFF(14));
  MLP(3);
  K_PAGE(7, cb2, cb3, ISSUE, cb0, cb1, rv, COFF(0));
  float m = fmaxf(fmaxf(fmaxf(sc[0], sc[1]), fmaxf(sc[2], sc[3])), fmaxf(fmaxf(sc[4], sc[5]), fmaxf(sc[6], sc[7])));
  m = fmaxf(m, dpp<XOR2>(m));
  m = fmaxf(m, dpp<ROR4>(m));
  m = fmaxf(m, dpp<ROR8>(m));
  m *= SC2;
  float l = 0.f;
#pragma unroll
  for (int k = 0; k < 8; ++k) { sc[k] = ex2(fmaf(sc[k], SC2, -m)); l += sc[k]; }
  float a0[8], a1[8];
#pragma unroll
  for (int k = 0; k < 8; ++k) { a0[k] = 0.f; a1[k] = 0.f; }
  MLP(4);
#pragma unroll 1
  for (int i = 0; i < NPG; i += 2) {
    const int so = wpo + (i + 1) * PSTR;
    V_PAGE(sc[0], cb0, cb1, cb2, cb3, so);
    MLP(5 + (i >> 1));
    V_PAGE(sc[1], cb2, cb3, cb0, cb1, so + PSTR);
#pragma unroll
    for (int k = 0; k < 6; ++k) sc[k] = sc[k + 2];
  }

  {
    const float mw_ = xrow_max(m);
    const float sc_ = ex2(m - mw_);
    float lr = l * sc_;
    lr += dpp<XOR2>(lr);
    lr += dpp<ROR4>(lr);
    lr += dpp<ROR8>(lr);
    lr = xrow_sum(lr);
    const float s0 = dpp<QP0>(sc_), s1 = dpp<QP1>(sc_);
    float t8[8];
#pragma unroll
    for (int k = 0; k < 8; ++k) t8[k] = swap32_add(a0[k] * s0, a1[k] * s1);
    f32x4 t4;
#pragma unroll
    for (int k = 0; k < 4; ++k) t4[k] = swap16_add(t8[k], t8[k + 4]);
    float* rec = ldf + L_MW + w * MW_W;
    *reinterpret_cast<f32x4*>(rec + (lane >> 5) * 128 + c * 8 + (r & 1) * 4) = t4;
    if (lane < 2) { rec[256 + lane * 2] = mw_; rec[257 + lane * 2] = lr; }
  }
  LDS_BARRIER();
  if (w < 2) {
    float2 ml[NWAVE], ov[NWAVE];
#pragma unroll
    for (int ww = 0; ww < NWAVE; ++ww) ml[ww] = *reinterpret_cast<const float2*>(ldf + L_MW + ww * MW_W + 256 + w * 2);
#pragma unroll
    for (int ww = 0; ww < NWAVE; ++ww) ov[ww] = *reinterpret_cast<const float2*>(ldf + L_MW + ww * MW_W + w * 128 + lane * 2);
    SB();
    float M = -INFINITY;
#pragma unroll
    for (int ww = 0; ww < NWAVE; ++ww) M = fmaxf(M, ml[ww].x);
    float O0 = 0.f, O1 = 0.f, L = 0.f;
#pragma unroll
    for (int ww = 0; ww < NWAVE; ++ww) {
      const float wgt = ex2(ml[ww].x - M);
      L = fmaf(wgt, ml[ww].y, L);
      O0 = fmaf(wgt, ov[ww].x, O0); O1 = fmaf(wgt, ov[ww].y, O1);
    }
    const int ro = WS_OG + (prod * 2 + w) * OREC;
    const u32x4 go = {tag, __float_as_uint(O0), tag, __float_as_uint(O1)};
    __builtin_amdgcn_raw_buffer_store_b128(go, rs, lo16, ro, AUX_SC1);
    const u32x4 gm = {tag, __float_as_uint(M), tag, __float_as_uint(L)};
    __builtin_amdgcn_raw_buffer_store_b128(gm, rs, lane == 0 ? 1024 : 0x7fffffff, ro, AUX_SC1);
  }
  {
    const int ro = WS_OG + (((w >> 1) * 64 + (h1 >> 1) * 4) * 2 + (h1 & 1)) * OREC;
    const int vo = r * 2 * OREC + ((w & 1) * 64 + c * 2) * 8;
    u32x4 g0, g1, gm;
    g0 = __builtin_amdgcn_raw_buffer_load_b128(rs, vo, ro, AUX_SC1);
    g1 = __builtin_amdgcn_raw_buffer_load_b128(rs, vo + 256, ro, AUX_SC1);
    gm = __builtin_amdgcn_raw_buffer_load_b128(rs, r * 2 * OREC + 1024, ro, AUX_SC1);
    SB();
    const float ysum = wave_treduce16(ya, lane);
    if ((lane & 3) == 0) ldf[L_Y + w * 16 + (lane >> 2)] = ysum;
    u32x4 bfr[4];
    if (w < 5) {
      const unsigned short* wts = reinterpret_cast<const unsigned short*>(lds + L_WT) + (r * 8) * CB + w * 16 + c;
#pragma unroll
      for (int kk = 0; kk < 4; ++kk)
#pragma unroll
        for (int j = 0; j < 4; ++j)
          bfr[kk][j] = (unsigned)wts[(kk * 32 + 2 * j) * CB] | ((unsigned)wts[(kk * 32 + 2 * j + 1) * CB] << 16);
    }
    SB();
    bool ok = (g0[0] == tag) & (g0[2] == tag) & (g1[0] == tag) & (g1[2] == tag) & (gm[0] == tag) & (gm[2] == tag);
#pragma unroll 1
    for (unsigned spins = 0; spins < SPIN_CAP; ++spins) {
      const unsigned long long miss = __ballot(!ok);
      if (miss == 0) break;
      if (__popcll(miss) > 16) __builtin_amdgcn_s_sleep(16);
      if (!ok) {
        g0 = __builtin_amdgcn_raw_buffer_load_b128(rs, vo, ro, AUX_SC1);
        g1 = __builtin_amdgcn_raw_buffer_load_b128(rs, vo + 256, ro, AUX_SC1);
        gm = __builtin_amdgcn_raw_buffer_load_b128(rs, r * 2 * OREC + 1024, ro, AUX_SC1);
        ok = (g0[0] == tag) & (g0[2] == tag) & (g1[0] == tag) & (g1[2] == tag) & (gm[0] == tag) & (gm[2] == tag);
      }
      asm volatile("" ::: "memory");
    }
    const float Ms = __uint_as_float(gm[1]);
    const float wgt = ex2(Ms - xrow_max(Ms));
    const float Ls = xrow_sum(wgt * __uint_as_float(gm[3]));
    const float t0 = swap32_add(wgt * __uint_as_float(g0[1]), wgt * __uint_as_float(g1[1]));
    const float t1 = swap32_add(wgt * __uint_as_float(g0[3]), wgt * __uint_as_float(g1[3]));
    const float ov = swap16_add(t0, t1) * rcp(Ls);
    reinterpret_cast<unsigned short*>(lds + L_O)[(w >> 1) * DH + (w & 1) * 64 + (r >> 1) * 32 + c * 2 + (r & 1)] = (unsigned short)(cvtpk(ov, 0.f) & 0xffffu);
    LDS_BARRIER();
    if (w < 5) {
      f32x4 acc0 = {0.f, 0.f, 0.f, 0.f}, acc1 = {0.f, 0.f, 0.f, 0.f};
      u32x4 afr[4];
#pragma unroll
      for (int kk = 0; kk < 4; ++kk) afr[kk] = *reinterpret_cast<const u32x4*>(lds + L_O + (c & 3) * (DH / 2) + kk * 16 + r * 4);
      acc0 = __builtin_amdgcn_mfma_f32_16x16x32_bf16(__builtin_bit_cast(bf16x8, bfr[0]), __builtin_bit_cast(bf16x8, afr[0]), acc0, 0, 0, 0);
      acc1 = __builtin_amdgcn_mfma_f32_16x16x32_bf16(__builtin_bit_cast(bf16x8, bfr[1]), __builtin_bit_cast(bf16x8, afr[1]), acc1, 0, 0, 0);
      acc0 = __builtin_amdgcn_mfma_f32_16x16x32_bf16(__builtin_bit_cast(bf16x8, bfr[2]), __builtin_bit_cast(bf16x8, afr[2]), acc0, 0, 0, 0);
      acc1 = __builtin_amdgcn_mfma_f32_16x16x32_bf16(__builtin_bit_cast(bf16x8, bfr[3]), __builtin_bit_cast(bf16x8, afr[3]), acc1, 0, 0, 0);
      const int so = (c < 8 && w * 16 + r * 4 < CB) ? (c & 3) * SROW + r * 32 + (c >> 2) * 16 : 0x7fffffff;
      const int sb = WS_SG + (h1 * 8 + cbk) * SREC + w * 128;
      const float e0 = (c & 4) ? acc0[2] + acc1[2] : acc0[0] + acc1[0], e1 = (c & 4) ? acc0[3] + acc1[3] : acc0[1] + acc1[1];
      const u32x4 s0 = {tag, __float_as_uint(e0), tag, __float_as_uint(e1)};
      __builtin_amdgcn_raw_buffer_store_b128(s0, rs, so, sb, AUX_SC1);
    } else {
      const int odd = prod & 1, pr = 2 * (w - 5) + (r >> 1);
      const int j0 = 2 * pr - odd;
      const bool v0 = j0 >= 0 && j0 < NOUT, v1 = j0 + 1 < NOUT;
      const int so = WS_SG + ((prod & 63) >> 3) * SREC + b * SROW + ((prod & 7) * NOUT - odd) * 8;
      const int vo2 = pr < 5 ? ((r & 1) * 16 + c) * 8 * SREC + pr * 16 : 0x7fffffff;
      u32x4 gs = __builtin_amdgcn_raw_buffer_load_b128(rs, vo2, so, AUX_SC1);
      SB();
      const int jj = min(max(j0 + (c & 1), 0), NOUT - 1);
      float y = ldf[L_FLAG + 2];
#pragma unroll
      for (int ww = 0; ww < NWAVE; ++ww) y += ldf[L_Y + ww * 16 + jj];
      float g = y / (1.0f + __expf(-y));
      float xo = ldf[L_X + col0 + jj];
      asm volatile("" : "+v"(g), "+v"(xo));
      SB();
      bool ok2 = (pr >= 5) | ((gs[0] == tag) & (gs[2] == tag));
#pragma unroll 1
      for (unsigned spins = 0; spins < SPIN_CAP; ++spins) {
        if (__all(ok2)) break;
        if (!ok2) {
          gs = __builtin_amdgcn_raw_buffer_load_b128(rs, vo2, so, AUX_SC1);
          ok2 = (gs[0] == tag) & (gs[2] == tag);
        }
        asm volatile("" ::: "memory");
      }
      const float e0 = __uint_as_float(gs[1]), e1 = __uint_as_float(gs[3]);
      const float u0 = e0 + dpp<XOR1>(e0), u1 = e1 + dpp<XOR1>(e1);
      float s = (c & 1) ? u1 : u0;
      s += dpp<XOR2>(s);
      s += dpp<ROR4>(s);
      s += dpp<ROR8>(s);
      s = swap16_add(s, s);
      if ((r & 1) == 0 && c < 2 && (c ? v1 : v0)) out[b * DM + col0 + j0 + c] = (unsigned short)(cvtpk(fmaf(s, g, xo), 0.f) & 0xffffu);
      if (bid == 0 && w == 5 && lane == 0) __hip_atomic_store(reinterpret_cast<unsigned*>(ws + WS_EPOCH), tag, RLX_AGENT);
    }
  }
}

}

extern "C" void kernel_launch(void* const* d_in, const int* in_sizes, int n_in, void* d_out, int out_size, void* d_ws, size_t ws_size,
                              hipStream_t stream) {
  (void)in_sizes; (void)n_in; (void)out_size; (void)ws_size;
  fused_decode_kernel<<<NWG, NTHR, 0, stream>>>(
      (const unsigned short*)d_in[0], (const unsigned short*)d_in[1], (const unsigned short*)d_in[2], (const unsigned short*)d_in[3],
      (const unsigned short*)d_in[4], (const unsigned short*)d_in[6], (const unsigned short*)d_in[7], (const unsigned short*)d_in[8],
      (const unsigned short*)d_in[9], (unsigned short*)d_out, (unsigned char*)d_ws);
}
```

```cpp
#include <hip/hip_runtime.h>
#include <hip/hip_bf16.h>
#include <stdint.h>

#define PACE 4
#define CPF 128
#define SLP 0
#define SINIT 2
#define HPRE 2
#define NOWQ 0
#define NOWO 0
#define WOAUX AUX_NT
#define WQAUX AUX_NT
#define STR_(x) #x
#define STR(x) STR_(x)
namespace {

constexpr int NB = 4, DM = 576, HQ = 32, HKV = 16, SK = 8192, DH = 128, FF = 4608;
constexpr int NWG = 256, NTHR = 512, NWAVE = 8;
constexpr int NSPLIT = 4;
constexpr int KEYS_WG = SK / NSPLIT;
constexpr int PAGE = 32;
constexpr int NPG = KEYS_WG / PAGE / NWAVE;
constexpr int QN = HQ * DH;
constexpr float LOG2E = 1.4426950408889634f;
constexpr float SC2 = 0.08838834764831845f * LOG2E;
constexpr float EPS = 1e-5f;
constexpr int DEPTH = 2;
constexpr int CB = 72;
constexpr int NOUT = 9;

constexpr int WS_EPOCH = 0;
constexpr int WS_QG = 4096;
constexpr int WS_OG = WS_QG + NB * 4 * QN * 8;
constexpr int OREC = 1152;
constexpr int WS_SG = WS_OG + NWG * 2 * OREC;
constexpr int SROW = 640, SREC = NB * SROW;
constexpr int WS_BYTES = WS_SG + HQ * 8 * SREC;
constexpr unsigned TAG_STEP = 0x9E3779B1u;
constexpr int CODE_BYTES = 18432;
constexpr unsigned SPIN_CAP = 1u << 20;

constexpr int L_XN = 0;
constexpr int WQ_ROW = 40;
constexpr int L_WQ = L_XN + NB * 320;
constexpr int L_QB = L_WQ + 160 * WQ_ROW;
constexpr int L_FLAG = L_QB + 128;
constexpr int L_H = L_FLAG + 8;
constexpr int L_X = L_H + 640;
constexpr int L_MW = L_X + 320;
constexpr int MW_W = 3 * 288;
constexpr int L_WT = L_MW + NWAVE * MW_W;
constexpr int L_O = L_WT + DH * CB / 2 + 8;
constexpr int L_Y = L_O + NB * DH / 2;
constexpr int L_WORDS = L_Y + NWAVE * 16;
static_assert(L_WORDS * 4 <= 163840, "LDS");
static_assert((L_WQ % 4 | L_QB % 4 | L_MW % 4 | L_WT % 4 | L_O % 4 | L_FLAG % 4 | L_H % 2) == 0, "align");

typedef unsigned u32x4 __attribute__((ext_vector_type(4)));
typedef unsigned u32x2 __attribute__((ext_vector_type(2)));
typedef float f32x4 __attribute__((ext_vector_type(4)));
typedef __bf16 bf16x2 __attribute__((ext_vector_type(2)));
typedef __bf16 bf16x8 __attribute__((ext_vector_type(8)));
typedef short s16x4 __attribute__((ext_vector_type(4)));
#define RLX_AGENT __ATOMIC_RELAXED, __HIP_MEMORY_SCOPE_AGENT
constexpr int AUX_SC1 = 16, AUX_NT = 2, AUX_KV = 18;

__device__ __forceinline__ float dot2(unsigned a, unsigned b, float acc) {
  return __builtin_amdgcn_fdot2_f32_bf16(__builtin_bit_cast(bf16x2, a), __builtin_bit_cast(bf16x2, b), acc, false);
}
__device__ __forceinline__ unsigned cvtpk(float lo, float hi) {
  bf16x2 r = {(__bf16)lo, (__bf16)hi};
  return __builtin_bit_cast(unsigned, r);
}
__device__ __forceinline__ float bf_lo(unsigned v) { return __builtin_bit_cast(float, v << 16); }
__device__ __forceinline__ float bf_hi(unsigned v) { return __builtin_bit_cast(float, v & 0xffff0000u); }
__device__ __forceinline__ float bf2f(unsigned short v) { return __builtin_bit_cast(float, (unsigned)v << 16); }
template <int CTRL> __device__ __forceinline__ float dpp(float x) {
  return __builtin_bit_cast(float, __builtin_amdgcn_mov_dpp(__builtin_bit_cast(int, x), CTRL, 0xf, 0xf, true));
}
constexpr int XOR1 = 0xB1, XOR2 = 0x4E, XOR7 = 0x141, ROR8 = 0x128, ROR4 = 0x124, BCAST = 0x150;
constexpr int QP0 = 0x00, QP1 = 0x55;
__device__ __forceinline__ float ex2(float x) { return __builtin_amdgcn_exp2f(x); }
__device__ __forceinline__ float rcp(float x) { return __builtin_amdgcn_rcpf(x); }

__device__ __forceinline__ float swap32_add(float a, float b) {
  auto s = __builtin_amdgcn_permlane32_swap(__float_as_uint(a), __float_as_uint(b), false, false);
  return __uint_as_float(s[0]) + __uint_as_float(s[1]);
}
__device__ __forceinline__ float swap16_add(float a, float b) {
  auto s = __builtin_amdgcn_permlane16_swap(__float_as_uint(a), __float_as_uint(b), false, false);
  return __uint_as_float(s[0]) + __uint_as_float(s[1]);
}
__device__ __forceinline__ float xrow_max(float x) {
  auto s = __builtin_amdgcn_permlane16_swap(__float_as_uint(x), __float_as_uint(x), false, false);
  x = fmaxf(__uint_as_float(s[0]), __uint_as_float(s[1]));
  auto t = __builtin_amdgcn_permlane32_swap(__float_as_uint(x), __float_as_uint(x), false, false);
  return fmaxf(__uint_as_float(t[0]), __uint_as_float(t[1]));
}
__device__ __forceinline__ float xrow_sum(float x) {
  x = swap16_add(x, x);
  return swap32_add(x, x);
}
__device__ __forceinline__ float row_sum(float x) {
  x += dpp<XOR1>(x);
  x += dpp<XOR2>(x);
  x += dpp<XOR7>(x);
  x += dpp<ROR8>(x);
  return x;
}
__device__ __forceinline__ float wave_sum(float x) { return xrow_sum(row_sum(x)); }
__device__ __forceinline__ float wave_treduce16(float (&v)[16], int lane) {
#pragma unroll
  for (int j = 0; j < 8; ++j) v[j] = swap32_add(v[j], v[j + 8]);
#pragma unroll
  for (int j = 0; j < 4; ++j) v[j] = swap16_add(v[j], v[j + 4]);
#pragma unroll
  for (int j = 0; j < 2; ++j) {
    const float a = v[j] + dpp<ROR8>(v[j]), b = v[j + 2] + dpp<ROR8>(v[j + 2]);
    v[j] = (lane & 8) ? b : a;
  }
  {
    const float a = v[0] + dpp<XOR7>(v[0]), b = v[1] + dpp<XOR7>(v[1]);
    v[0] = (lane & 4) ? b : a;
  }
  float r = v[0];
  r += dpp<XOR2>(r);
  r += dpp<XOR1>(r);
  return r;
}

#define SB() __builtin_amdgcn_sched_barrier(0)
#define PIN8(a) asm volatile("" : "+v"(a[0]), "+v"(a[1]), "+v"(a[2]), "+v"(a[3]), "+v"(a[4]), "+v"(a[5]), "+v"(a[6]), "+v"(a[7]) : : "memory")
#define LDS_BARRIER() do { asm volatile("s_waitcnt lgkmcnt(0)" ::: "memory"); __builtin_amdgcn_s_barrier(); asm volatile("" ::: "memory"); } while (0)

__global__ void __launch_bounds__(NTHR, 2)
fused_decode_kernel(const unsigned short* __restrict__ x, const unsigned short* __restrict__ kc, const unsigned short* __restrict__ vc,
                    const unsigned short* __restrict__ wq, const unsigned short* __restrict__ wo, const unsigned short* __restrict__ w1,
                    const unsigned short* __restrict__ b1, const unsigned short* __restrict__ w2, const unsigned short* __restrict__ b2,
                    unsigned short* __restrict__ out, unsigned char* ws) {
  __shared__ __attribute__((aligned(16))) unsigned lds[L_WORDS];
  float* ldf = reinterpret_cast<float*>(lds);
  const int tid = threadIdx.x, lane = tid & 63, w = __builtin_amdgcn_readfirstlane(tid >> 6);
  const int r = lane >> 4, c = lane & 15;
  unsigned cpf;
  {
    unsigned long long pc0;
    asm volatile("s_getpc_b64 %0" : "=s"(pc0));
    const __amdgpu_buffer_rsrc_t rc = __builtin_amdgcn_make_buffer_rsrc((void*)(pc0 & ~127ull), 0, CODE_BYTES, 0x00020000);
    cpf = __builtin_amdgcn_raw_buffer_load_b32(rc, lane * CPF, (7 - w) * 64 * CPF, 0);
  }
  asm volatile("" :: "s"(x), "s"(kc), "s"(vc), "s"(wq), "s"(wo), "s"(w1), "s"(b1), "s"(w2), "s"(b2), "s"(out), "s"(ws));
  const int bid = blockIdx.x, xcd = bid & 7, ii = bid >> 3;
  const int kvh = ii & 15, b = (xcd >> 1) & 3, sp = ((xcd & 1) << 1) | (ii >> 4);
  const int prod = b * 64 + kvh * 4 + sp;
  const int col0 = (prod & 63) * NOUT;
  const int h1 = 4 * xcd + (ii >> 3), cbk = ii & 7;

  const unsigned tag = *reinterpret_cast<const __attribute__((address_space(4))) unsigned*>(reinterpret_cast<uintptr_t>(ws + WS_EPOCH)) + TAG_STEP;
  const __amdgpu_buffer_rsrc_t rx = __builtin_amdgcn_make_buffer_rsrc((void*)x, 0, NB * DM * 2, 0x00020000);
  const __amdgpu_buffer_rsrc_t rb2 = __builtin_amdgcn_make_buffer_rsrc((void*)b2, 0, 2, 0x00020000);
  const size_t kvbase = (size_t)(b * HKV + kvh) * SK * DH;
  const __amdgpu_buffer_rsrc_t rk = __builtin_amdgcn_make_buffer_rsrc((void*)(kc + kvbase), 0, SK * DH * 2, 0x00020000);
  const __amdgpu_buffer_rsrc_t rv = __builtin_amdgcn_make_buffer_rsrc((void*)(vc + kvbase), 0, SK * DH * 2, 0x00020000);
  const __amdgpu_buffer_rsrc_t rwq = __builtin_amdgcn_make_buffer_rsrc((void*)wq, 0, DM * QN * 2, 0x00020000);
  const __amdgpu_buffer_rsrc_t rwo = __builtin_amdgcn_make_buffer_rsrc((void*)wo, 0, DM * QN * 2, 0x00020000);
  const __amdgpu_buffer_rsrc_t rs = __builtin_amdgcn_make_buffer_rsrc((void*)ws, 0, WS_BYTES, 0x00020000);
  const int lo16 = lane * 16;
  const int wpo = ((w & 3) << 12) | ((sp >> 1) << 14) | ((w >> 2) << 18) | ((sp & 1) << 19);
  constexpr int HALF = 1 << 15;
#define POFF(p) (wpo + (((p) & 3) << 16) + (((p) >> 2) << 20))
#define COFF(n) (POFF((n) >> 1) + ((n) & 1) * HALF)
#define LDC(rs_, n, t) __builtin_amdgcn_raw_buffer_load_b128(rs_, lo16 + (t) * 1024, COFF(n), AUX_KV)
  u32x4 cb0[4], cb1[4], cb2[4], cb3[4], cb4[4], cb5[4], cb6[4], cb7[4];
  const int cb = bid & 63, ks = bid >> 6;
  unsigned xr[5], xn[5];
  int xo_[5], wqa_, qso_;
#define LD01() do { _Pragma("unroll") for (int t = 0; t < 4; ++t) { cb0[t] = LDC(rk, 0, t); SB(); } \
    _Pragma("unroll") for (int t = 0; t < 4; ++t) { cb1[t] = LDC(rk, 1, t); SB(); } } while (0)
  if (w < 4) {
    u32x4 wqr[5];
    const int oob = lane < 32 ? 0 : 0x7fff0000;
#pragma unroll
    for (int j = 0; j < 5; ++j) xr[j] = __builtin_amdgcn_raw_buffer_load_b32(rx, lane * 4 + j * 256 + (j == 4 ? oob : 0), w * DM * 2, 0);
    const int wqv = (lane >> 3) * QN * 2 + (lane & 7) * 16;
    const int wqs = ((ks * 144 + w * 36) * QN + cb * 64) * 2;
#pragma unroll
    for (int j = 0; j < 5; ++j) wqr[j] = __builtin_amdgcn_raw_buffer_load_b128(rwq, wqv + (j == 4 ? oob : 0), wqs + j * 8 * QN * 2 + NOWQ * 0x10000000, WQAUX);
    SB();
#pragma unroll
    for (int k = 0; k < 5; ++k) {
      const int G = 4 * k + r, blk = (G * 13) >> 6;
      xo_[k] = (L_XN + (c & 3) * 320 + ks * 72 + blk * 18 + (G - 5 * blk) * 4) * 4;
      asm volatile("" : "+v"(xo_[k]));
    }
    wqa_ = ((8 * r + (c >> 2)) * WQ_ROW + w * 8 + (c & 3) * 2) * 4;
    qso_ = c < 8 ? (c & 3) * 4 * QN * 8 + r * 32 + (c >> 2) * 16 : 0x7fffffff;
    asm volatile("" : "+v"(wqa_), "+v"(qso_));
    SB();
    float xv[10];
    float s = 0.f;
#pragma unroll
    for (int j = 0; j < 5; ++j) { xv[2 * j] = bf_lo(xr[j]); xv[2 * j + 1] = bf_hi(xr[j]); s += xv[2 * j] + xv[2 * j + 1]; }
    const float mean = wave_sum(s) * (1.0f / DM);
    float v = 0.f;
#pragma unroll
    for (int j = 0; j < 10; ++j) { xv[j] = (j < 8 || lane < 32) ? xv[j] - mean : 0.f; v = fmaf(xv[j], xv[j], v); }
    const float rstd = __builtin_amdgcn_rsqf(wave_sum(v) * (1.0f / DM) + EPS);
#pragma unroll
    for (int j = 0; j < 5; ++j) { xn[j] = cvtpk(xv[2 * j] * rstd, xv[2 * j + 1] * rstd); lds[L_XN + w * 320 + j * 64 + lane] = xn[j]; }
    SB();
#pragma unroll
    for (int j = 0; j < 5; ++j) *reinterpret_cast<u32x4*>(lds + L_WQ + (w * 40 + j * 8 + (lane >> 3)) * WQ_ROW + (lane & 7) * 4) = wqr[j];
  } else {
    LD01();
    if (w == 5 && lane < 4) lds[L_FLAG + lane] = 0u;
  }
  LDS_BARRIER();
  if (w < 4) {
    typedef __attribute__((address_space(3))) s16x4* lds_s16x4;
    const unsigned* wqa = reinterpret_cast<const unsigned*>(reinterpret_cast<const char*>(lds + L_WQ) + wqa_);
    f32x4 acc0 = {0.f, 0.f, 0.f, 0.f}, acc1 = {0.f, 0.f, 0.f, 0.f};
    u32x4 af[5], bf[5];
#pragma unroll
    for (int k = 0; k < 5; ++k) {
      const u32x2 lo = __builtin_bit_cast(u32x2, __builtin_amdgcn_ds_read_tr16_b64_v4i16((lds_s16x4)(wqa + (32 * k) * WQ_ROW)));
      const u32x2 hi = __builtin_bit_cast(u32x2, __builtin_amdgcn_ds_read_tr16_b64_v4i16((lds_s16x4)(wqa + (32 * k + 4) * WQ_ROW)));
      af[k] = u32x4{lo[0], lo[1], hi[0], hi[1]};
      const unsigned* xp = reinterpret_cast<const unsigned*>(reinterpret_cast<const char*>(lds) + xo_[k]);
      const u32x2 x0 = *reinterpret_cast<const u32x2*>(xp), x1 = *reinterpret_cast<const u32x2*>(xp + 2);
      bf[k] = u32x4{x0[0], x0[1], x1[0], x1[1]};
    }
    SB();
#pragma unroll
    for (int k = 0; k < 5; ++k) {
      if (k & 1) acc1 = __builtin_amdgcn_mfma_f32_16x16x32_bf16(__builtin_bit_cast(bf16x8, af[k]), __builtin_bit_cast(bf16x8, bf[k]), acc1, 0, 0, 0);
      else acc0 = __builtin_amdgcn_mfma_f32_16x16x32_bf16(__builtin_bit_cast(bf16x8, af[k]), __builtin_bit_cast(bf16x8, bf[k]), acc0, 0, 0, 0);
    }
    const float e0 = (c & 4) ? acc0[2] + acc1[2] : acc0[0] + acc1[0], e1 = (c & 4) ? acc0[3] + acc1[3] : acc0[1] + acc1[1];
    const u32x4 gq = {tag, __float_as_uint(e0), tag, __float_as_uint(e1)};
    __builtin_amdgcn_raw_buffer_store_b128(gq, rs, qso_, WS_QG + (ks * QN + cb * 64 + w * 16) * 8, AUX_SC1);
    SB();
    LD01();
    if (w == b) {
      float xv[10];
      float s2 = 0.f;
#pragma unroll
      for (int j = 0; j < 5; ++j) { xv[2 * j] = bf_lo(xn[j]); xv[2 * j + 1] = bf_hi(xn[j]); s2 += xv[2 * j] + xv[2 * j + 1]; lds[L_X + j * 64 + lane] = xr[j]; }
      const float mean2 = wave_sum(s2) * (1.0f / DM);
      float v2 = 0.f;
#pragma unroll
      for (int j = 0; j < 10; ++j) { xv[j] = (j < 8 || lane < 32) ? xv[j] - mean2 : 0.f; v2 = fmaf(xv[j], xv[j], v2); }
      const float rstd2 = __builtin_amdgcn_rsqf(wave_sum(v2) * (1.0f / DM) + EPS);
#pragma unroll
      for (int j = 0; j < 5; ++j) *reinterpret_cast<float2*>(ldf + L_H + j * 128 + lane * 2) = make_float2(xv[2 * j] * rstd2, xv[2 * j + 1] * rstd2);
    }
    const int qo = WS_QG + ((b * 4 + (lane >> 5) * 2) * QN + kvh * 256 + w * 64 + (lane & 31) * 2) * 8;
    u32x4 gv[2];
    bool ok = false;
#pragma unroll 1
    for (unsigned spins = 0; spins < SPIN_CAP; ++spins) {
      if (!ok) {
#pragma unroll
        for (int s = 0; s < 2; ++s) gv[s] = __builtin_amdgcn_raw_buffer_load_b128(rs, qo, s * QN * 8, AUX_SC1);
        ok = (gv[0][0] == tag) & (gv[0][2] == tag) & (gv[1][0] == tag) & (gv[1][2] == tag);
      }
      if (__all(ok)) break;
      asm volatile("" ::: "memory");
    }
    const float q0 = __uint_as_float(gv[0][1]) + __uint_as_float(gv[1][1]), q1 = __uint_as_float(gv[0][3]) + __uint_as_float(gv[1][3]);
    const float qa = swap32_add(q0, q0), qb = swap32_add(q1, q1);
    if (lane < 32) lds[L_QB + w * 32 + lane] = cvtpk(qa, qb);
    asm volatile("" ::: "memory");
    if (lane == 0) __hip_atomic_store(lds + L_FLAG + w, 1u, __ATOMIC_RELAXED, __HIP_MEMORY_SCOPE_WORKGROUP);
    asm volatile("" ::: "memory");
  }
  auto q_ready = [&]() {
    const unsigned long long f = __hip_atomic_load(reinterpret_cast<unsigned long long*>(lds + L_FLAG), __ATOMIC_RELAXED, __HIP_MEMORY_SCOPE_WORKGROUP) &
                                 __hip_atomic_load(reinterpret_cast<unsigned long long*>(lds + L_FLAG + 2), __ATOMIC_RELAXED, __HIP_MEMORY_SCOPE_WORKGROUP);
    return __builtin_amdgcn_readfirstlane((unsigned)f & (unsigned)(f >> 32)) != 0;
  };
  bool ready = false;
#define CHUNK(dst, n) do { SB(); _Pragma("unroll") for (int t = 0; t < 4; ++t) dst[t] = LDC(rk, n, t); SB(); \
    if (!ready) { asm volatile("s_waitcnt vmcnt(" STR(PACE) ")" ::: "memory"); ready = q_ready(); } SB(); } while (0)
  CHUNK(cb2, 2);
  CHUNK(cb3, 3);
  CHUNK(cb4, 4);
  CHUNK(cb5, 5);
  CHUNK(cb6, 6);
#pragma unroll
  for (int t = 0; t < 4; ++t) cb7[t] = LDC(rk, 7, t);
  SB();
  u32x4 mw[4], wt[3];
  const unsigned short b2r = __builtin_amdgcn_raw_buffer_load_b16(rb2, 0, 0, 0);
#pragma unroll
  for (int j = 0; j < 3; ++j) {
    const unsigned short* src = j == 0 ? w1 : (j == 1 ? b1 : w2);
    mw[j] = reinterpret_cast<const u32x4*>(src)[72 * w + lane];
  }
  {
    const int a = lane >> 3;
    const unsigned short* src = a == 0 ? w1 : (a == 1 ? b1 : w2);
    mw[3] = reinterpret_cast<const u32x4*>(src)[72 * w + 64 + (lane & 7)];
  }
#pragma unroll
  for (int j = 0; j < 3; ++j) {
    const int ch = tid + NTHR * j;
    const int row = (ch * 7282) >> 16;
    const int vo = row * (DM * 2) + (ch - row * 9) * 16;
    wt[j] = __builtin_amdgcn_raw_buffer_load_b128(rwo, (j < 2 || tid < 128) ? vo : 0x7fffffff, (h1 * DH * DM + cbk * CB) * 2 + NOWO * 0x10000000, WOAUX);
  }
  SB();
  while (!ready) { __builtin_amdgcn_s_sleep(1); ready = q_ready(); }
  asm volatile("" ::: "memory");
  asm volatile("" :: "v"(cpf));
  const u32x4 qh0 = *reinterpret_cast<const u32x4*>(lds + L_QB + c * 4);
  const u32x4 qh1 = *reinterpret_cast<const u32x4*>(lds + L_QB + 64 + c * 4);
  float hv[NOUT];
  float ya[16];
#pragma unroll
  for (int j = 0; j < 16; ++j) ya[j] = 0.f;
#pragma unroll
  for (int j = 0; j < NOUT; ++j) hv[j] = __builtin_bit_cast(float, __builtin_amdgcn_readfirstlane(lds[L_H + col0 + j]));
  const unsigned short* mwl = reinterpret_cast<const unsigned short*>(lds + L_MW + w * MW_W);

  auto mlp_slice = [&](int k) {
    const int f = k * 64 + lane;
    const float w1f = bf2f(mwl[f]), b1f = bf2f(mwl[576 + f]), w2f = bf2f(mwl[2 * 576 + f]);
    constexpr float C1 = -2.0f * 0.7978845608028654f * LOG2E, C2 = C1 * 0.044715f;
#pragma unroll
    for (int j = 0; j < NOUT; ++j) {
      const float uu = fmaf(hv[j], w1f, b1f);
      const float e = ex2(uu * fmaf(uu * uu, C2, C1));
      ya[j] = fmaf(uu * w2f, rcp(1.0f + e), ya[j]);
    }
  };

#define PINY() asm volatile("" : "+v"(ya[0]), "+v"(ya[1]), "+v"(ya[2]), "+v"(ya[3]), "+v"(ya[4]), "+v"(ya[5]), "+v"(ya[6]), "+v"(ya[7]), "+v"(ya[8]))
#define MLP(k) do { mlp_slice(k); PINY(); SB(); } while (0)
#define ISSUE(dst, rs_, so) do { _Pragma("unroll") for (int t = 0; t < 4; ++t) { \
      dst[t] = __builtin_amdgcn_raw_buffer_load_b128(rs_, lo16 + t * 1024, so, AUX_KV); SB(); } } while (0)
#define NOISSUE(dst, rs_, so) do {} while (0)
#define QK_HALF(kb, hf) _Pragma("unroll") for (int t = 0; t < 4; ++t) { \
      float d0 = 0.f, d1 = 0.f; \
      _Pragma("unroll") for (int k = 0; k < 4; ++k) { d0 = dot2(kb[t][k], qh0[k], d0); d1 = dot2(kb[t][k], qh1[k], d1); } \
      xs[8 * (hf) + 2 * t] = d0; xs[8 * (hf) + 2 * t + 1] = d1; }
#define TR_STEP(HF, CTRL, BIT) _Pragma("unroll") for (int h = 0; h < (HF); ++h) { \
      const float t1_ = xs[h] + dpp<CTRL>(xs[h]), t2_ = xs[h + (HF)] + dpp<CTRL>(xs[h + (HF)]); \
      xs[h] = (c & (BIT)) ? t2_ : t1_; }
#define K_PAGE(pg, blo, bhi, ISS, nlo, nhi, rs_, so) do { \
    float xs[16]; \
    QK_HALF(blo, 0) \
    PIN8(xs); SB(); \
    ISS(nlo, rs_, so); \
    QK_HALF(bhi, 1) \
    PIN8((xs + 8)); SB(); \
    ISS(nhi, rs_, (so) + HALF); \
    TR_STEP(8, ROR8, 8) TR_STEP(4, XOR7, 4) TR_STEP(2, XOR2, 2) TR_STEP(1, XOR1, 1) \
    sc[pg] = xs[0]; \
    asm volatile("" : "+v"(sc[pg])); SB(); \
  } while (0)
#define PV_STEP(vb, t, hf, p) { const float p0 = dpp<BCAST + 8 * (hf) + 2 * (t)>(p), p1 = dpp<BCAST + 8 * (hf) + 2 * (t) + 1>(p); \
      _Pragma("unroll") for (int k = 0; k < 4; ++k) { const float lo = bf_lo(vb[t][k]), hi = bf_hi(vb[t][k]); \
        a0[2 * k] = fmaf(p0, lo, a0[2 * k]); a0[2 * k + 1] = fmaf(p0, hi, a0[2 * k + 1]); \
        a1[2 * k] = fmaf(p1, lo, a1[2 * k]); a1[2 * k + 1] = fmaf(p1, hi, a1[2 * k + 1]); } }
#define V_PAGE(p, blo, bhi, nlo, nhi, so) do { \
    PV_STEP(blo, 0, 0, p) PV_STEP(blo, 1, 0, p) PV_STEP(blo, 2, 0, p) PV_STEP(blo, 3, 0, p) \
    PIN8(a0); PIN8(a1); SB(); \
    ISSUE(nlo, rv, so); \
    PV_STEP(bhi, 0, 1, p) PV_STEP(bhi, 1, 1, p) PV_STEP(bhi, 2, 1, p) PV_STEP(bhi, 3, 1, p) \
    PIN8(a0); PIN8(a1); SB(); \
    ISSUE(nhi, rv, (so) + HALF); \
  } while (0)
  float sc[8];
  K_PAGE(0, cb0, cb1, NOISSUE, cb2, cb3, rk, 0);
  K_PAGE(1, cb2, cb3, NOISSUE, cb4, cb5, rk, 0);
  K_PAGE(2, cb4, cb5, NOISSUE, cb6, cb7, rk, 0);
  K_PAGE(3, cb6, cb7, ISSUE, cb0, cb1, rk, COFF(8));
  {
    unsigned* mwd = lds + L_MW + w * MW_W;
#pragma unroll
    for (int j = 0; j < 3; ++j) *reinterpret_cast<u32x4*>(mwd + j * 288 + lane * 4) = mw[j];
    if (lane < 24) *reinterpret_cast<u32x4*>(mwd + (lane >> 3) * 288 + 256 + (lane & 7) * 4) = mw[3];
#pragma unroll
    for (int j = 0; j < 2; ++j) *reinterpret_cast<u32x4*>(lds + L_WT + (tid + NTHR * j) * 4) = wt[j];
    if (tid < 128) *reinterpret_cast<u32x4*>(lds + L_WT + (tid + NTHR * 2) * 4) = wt[2];
    if (tid == 0) ldf[L_FLAG + 4] = bf2f(b2r);
    asm volatile("" ::: "memory");
  }
  MLP(0);
  K_PAGE(4, cb0, cb1, ISSUE, cb2, cb3, rk, COFF(10));
  MLP(1);
  K_PAGE(5, cb2, cb3, ISSUE, cb0, cb1, rk, COFF(12));
  MLP(2);
  K_PAGE(6, cb0, cb1, ISSUE, cb2, cb3, rk, COFF(14));
  MLP(3);
  K_PAGE(7, cb2, cb3, ISSUE, cb0, cb1, rv, COFF(0));
  float m = fmaxf(fmaxf(fmaxf(sc[0], sc[1]), fmaxf(sc[2], sc[3])), fmaxf(fmaxf(sc[4], sc[5]), fmaxf(sc[6], sc[7])));
  m = fmaxf(m, dpp<XOR2>(m));
  m = fmaxf(m, dpp<ROR4>(m));
  m = fmaxf(m, dpp<ROR8>(m));
  m *= SC2;
  float l = 0.f;
#pragma unroll
  for (int k = 0; k < 8; ++k) { sc[k] = ex2(fmaf(sc[k], SC2, -m)); l += sc[k]; }
  float a0[8], a1[8];
#pragma unroll
  for (int k = 0; k < 8; ++k) { a0[k] = 0.f; a1[k] = 0.f; }
  MLP(4);
#pragma unroll 1
  for (int i = 0; i < NPG; i += 2) {
    V_PAGE(sc[0], cb0, cb1, cb2, cb3, POFF(i + 1));
    MLP(5 + (i >> 1));
    V_PAGE(sc[1], cb2, cb3, cb0, cb1, POFF(i + 2));
#pragma unroll
    for (int k = 0; k < 6; ++k) sc[k] = sc[k + 2];
  }

  {
    const float mw_ = xrow_max(m);
    const float sc_ = ex2(m - mw_);
    float lr = l * sc_;
    lr += dpp<XOR2>(lr);
    lr += dpp<ROR4>(lr);
    lr += dpp<ROR8>(lr);
    lr = xrow_sum(lr);
    const float s0 = dpp<QP0>(sc_), s1 = dpp<QP1>(sc_);
    float t8[8];
#pragma unroll
    for (int k = 0; k < 8; ++k) t8[k] = swap32_add(a0[k] * s0, a1[k] * s1);
    f32x4 t4;
#pragma unroll
    for (int k = 0; k < 4; ++k) t4[k] = swap16_add(t8[k], t8[k + 4]);
    float* rec = ldf + L_MW + w * MW_W;
    *reinterpret_cast<f32x4*>(rec + (lane >> 5) * 128 + c * 8 + (r & 1) * 4) = t4;
    if (lane < 2) { rec[256 + lane * 2] = mw_; rec[257 + lane * 2] = lr; }
  }
  LDS_BARRIER();
  if (w < 2) {
    float2 ml[NWAVE], ov[NWAVE];
#pragma unroll
    for (int ww = 0; ww < NWAVE; ++ww) ml[ww] = *reinterpret_cast<const float2*>(ldf + L_MW + ww * MW_W + 256 + w * 2);
#pragma unroll
    for (int ww = 0; ww < NWAVE; ++ww) ov[ww] = *reinterpret_cast<const float2*>(ldf + L_MW + ww * MW_W + w * 128 + lane * 2);
    SB();
    float M = -INFINITY;
#pragma unroll
    for (int ww = 0; ww < NWAVE; ++ww) M = fmaxf(M, ml[ww].x);
    float O0 = 0.f, O1 = 0.f, L = 0.f;
#pragma unroll
    for (int ww = 0; ww < NWAVE; ++ww) {
      const float wgt = ex2(ml[ww].x - M);
      L = fmaf(wgt, ml[ww].y, L);
      O0 = fmaf(wgt, ov[ww].x, O0); O1 = fmaf(wgt, ov[ww].y, O1);
    }
    const int ro = WS_OG + (prod * 2 + w) * OREC;
    const u32x4 go = {tag, __float_as_uint(O0), tag, __float_as_uint(O1)};
    __builtin_amdgcn_raw_buffer_store_b128(go, rs, lo16, ro, AUX_SC1);
    const u32x4 gm = {tag, __float_as_uint(M), tag, __float_as_uint(L)};
    __builtin_amdgcn_raw_buffer_store_b128(gm, rs, lane == 0 ? 1024 : 0x7fffffff, ro, AUX_SC1);
  }
  {
    const int ro = WS_OG + (((w >> 1) * 64 + (h1 >> 1) * 4) * 2 + (h1 & 1)) * OREC;
    const int vo = r * 2 * OREC + ((w & 1) * 64 + c * 2) * 8;
    u32x4 g0, g1, gm;
    g0 = __builtin_amdgcn_raw_buffer_load_b128(rs, vo, ro, AUX_SC1);
    g1 = __builtin_amdgcn_raw_buffer_load_b128(rs, vo + 256, ro, AUX_SC1);
    gm = __builtin_amdgcn_raw_buffer_load_b128(rs, r * 2 * OREC + 1024, ro, AUX_SC1);
    SB();
    const float ysum = wave_treduce16(ya, lane);
    if ((lane & 3) == 0) ldf[L_Y + w * 16 + (lane >> 2)] = ysum;
    u32x4 bfr[4];
    if (w < 5) {
      const unsigned short* wts = reinterpret_cast<const unsigned short*>(lds + L_WT) + (r * 8) * CB + w * 16 + c;
#pragma unroll
      for (int kk = 0; kk < 4; ++kk)
#pragma unroll
        for (int j = 0; j < 4; ++j)
          bfr[kk][j] = (unsigned)wts[(kk * 32 + 2 * j) * CB] | ((unsigned)wts[(kk * 32 + 2 * j + 1) * CB] << 16);
    }
    SB();
    bool ok = (g0[0] == tag) & (g0[2] == tag) & (g1[0] == tag) & (g1[2] == tag) & (gm[0] == tag) & (gm[2] == tag);
#pragma unroll 1
    for (unsigned spins = 0; spins < SPIN_CAP; ++spins) {
      const unsigned long long miss = __ballot(!ok);
      if (miss == 0) break;
      if (__popcll(miss) > 16) __builtin_amdgcn_s_sleep(16);
      if (!ok) {
        g0 = __builtin_amdgcn_raw_buffer_load_b128(rs, vo, ro, AUX_SC1);
        g1 = __builtin_amdgcn_raw_buffer_load_b128(rs, vo + 256, ro, AUX_SC1);
        gm = __builtin_amdgcn_raw_buffer_load_b128(rs, r * 2 * OREC + 1024, ro, AUX_SC1);
        ok = (g0[0] == tag) & (g0[2] == tag) & (g1[0] == tag) & (g1[2] == tag) & (gm[0] == tag) & (gm[2] == tag);
      }
      asm volatile("" ::: "memory");
    }
    const float Ms = __uint_as_float(gm[1]);
    const float wgt = ex2(Ms - xrow_max(Ms));
    const float Ls = xrow_sum(wgt * __uint_as_float(gm[3]));
    const float t0 = swap32_add(wgt * __uint_as_float(g0[1]), wgt * __uint_as_float(g1[1]));
    const float t1 = swap32_add(wgt * __uint_as_float(g0[3]), wgt * __uint_as_float(g1[3]));
    const float ov = swap16_add(t0, t1) * rcp(Ls);
    reinterpret_cast<unsigned short*>(lds + L_O)[(w >> 1) * DH + (w & 1) * 64 + (r >> 1) * 32 + c * 2 + (r & 1)] = (unsigned short)(cvtpk(ov, 0.f) & 0xffffu);
    LDS_BARRIER();
    if (w < 5) {
      f32x4 acc0 = {0.f, 0.f, 0.f, 0.f}, acc1 = {0.f, 0.f, 0.f, 0.f};
      u32x4 afr[4];
#pragma unroll
      for (int kk = 0; kk < 4; ++kk) afr[kk] = *reinterpret_cast<const u32x4*>(lds + L_O + (c & 3) * (DH / 2) + kk * 16 + r * 4);
      acc0 = __builtin_amdgcn_mfma_f32_16x16x32_bf16(__builtin_bit_cast(bf16x8, bfr[0]), __builtin_bit_cast(bf16x8, afr[0]), acc0, 0, 0, 0);
      acc1 = __builtin_amdgcn_mfma_f32_16x16x32_bf16(__builtin_bit_cast(bf16x8, bfr[1]), __builtin_bit_cast(bf16x8, afr[1]), acc1, 0, 0, 0);
      acc0 = __builtin_amdgcn_mfma_f32_16x16x32_bf16(__builtin_bit_cast(bf16x8, bfr[2]), __builtin_bit_cast(bf16x8, afr[2]), acc0, 0, 0, 0);
      acc1 = __builtin_amdgcn_mfma_f32_16x16x32_bf16(__builtin_bit_cast(bf16x8, bfr[3]), __builtin_bit_cast(bf16x8, afr[3]), acc1, 0, 0, 0);
      const int so = (c < 8 && w * 16 + r * 4 < CB) ? (c & 3) * SROW + r * 32 + (c >> 2) * 16 : 0x7fffffff;
      const int sb = WS_SG + (h1 * 8 + cbk) * SREC + w * 128;
      const float e0 = (c & 4) ? acc0[2] + acc1[2] : acc0[0] + acc1[0], e1 = (c & 4) ? acc0[3] + acc1[3] : acc0[1] + acc1[1];
      const u32x4 s0 = {tag, __float_as_uint(e0), tag, __float_as_uint(e1)};
      __builtin_amdgcn_raw_buffer_store_b128(s0, rs, so, sb, AUX_SC1);
    } else {
      const int odd = prod & 1, pr = 2 * (w - 5) + (r >> 1);
      const int j0 = 2 * pr - odd;
      const bool v0 = j0 >= 0 && j0 < NOUT, v1 = j0 + 1 < NOUT;
      const int so = WS_SG + ((prod & 63) >> 3) * SREC + b * SROW + ((prod & 7) * NOUT - odd) * 8;
      const int vo2 = pr < 5 ? ((r & 1) * 16 + c) * 8 * SREC + pr * 16 : 0x7fffffff;
      u32x4 gs = __builtin_amdgcn_raw_buffer_load_b128(rs, vo2, so, AUX_SC1);
      SB();
      const int jj = min(max(j0 + (c & 1), 0), NOUT - 1);
      float y = ldf[L_FLAG + 4];
#pragma unroll
      for (int ww = 0; ww < NWAVE; ++ww) y += ldf[L_Y + ww * 16 + jj];
      float g = y / (1.0f + __expf(-y));
      float xo = bf2f(reinterpret_cast<const unsigned short*>(lds + L_X)[col0 + jj]);
      asm volatile("" : "+v"(g), "+v"(xo));
      SB();
      bool ok2 = (pr >= 5) | ((gs[0] == tag) & (gs[2] == tag));
#pragma unroll 1
      for (unsigned spins = 0; spins < SPIN_CAP; ++spins) {
        if (__all(ok2)) break;
        if (!ok2) {
          gs = __builtin_amdgcn_raw_buffer_load_b128(rs, vo2, so, AUX_SC1);
          ok2 = (gs[0] == tag) & (gs[2] == tag);
        }
        asm volatile("" ::: "memory");
      }
      const float e0 = __uint_as_float(gs[1]), e1 = __uint_as_float(gs[3]);
      const float u0 = e0 + dpp<XOR1>(e0), u1 = e1 + dpp<XOR1>(e1);
      float s = (c & 1) ? u1 : u0;
      s += dpp<XOR2>(s);
      s += dpp<ROR4>(s);
      s += dpp<ROR8>(s);
      s = swap16_add(s, s);
      if ((r & 1) == 0 && c < 2 && (c ? v1 : v0)) out[b * DM + col0 + j0 + c] = (unsigned short)(cvtpk(fmaf(s, g, xo), 0.f) & 0xffffu);
      if (bid == 0 && w == 5 && lane == 0) __hip_atomic_store(reinterpret_cast<unsigned*>(ws + WS_EPOCH), tag, RLX_AGENT);
    }
  }
}

}

extern "C" void kernel_launch(void* const* d_in, const int* in_sizes, int n_in, void* d_out, int out_size, void* d_ws, size_t ws_size,
                              hipStream_t stream) {
  (void)in_sizes; (void)n_in; (void)out_size; (void)ws_size;
  fused_decode_kernel<<<NWG, NTHR, 0, stream>>>(
      (const unsigned short*)d_in[0], (const unsigned short*)d_in[1], (const unsigned short*)d_in[2], (const unsigned short*)d_in[3],
      (const unsigned short*)d_in[4], (const unsigned short*)d_in[6], (const unsigned short*)d_in[7], (const unsigned short*)d_in[8],
      (const unsigned short*)d_in[9], (unsigned short*)d_out, (unsigned char*)d_ws);
}
```

```cpp
#include <hip/hip_runtime.h>
#include <hip/hip_bf16.h>
#include <stdint.h>

#define STR_(x) #x
#define STR(x) STR_(x)

namespace {

constexpr int NB = 4, DM = 576, HQ = 32, HKV = 16, SK = 8192, DH = 128, FF = 4608;
constexpr int NWG = 256, NTHR = 512, NWAVE = 8;
constexpr int NSPLIT = 4;
constexpr int KEYS_WG = SK / NSPLIT;
constexpr int PAGE = 32;
constexpr int NPG = KEYS_WG / PAGE / NWAVE;
constexpr int QN = HQ * DH;
constexpr float LOG2E = 1.4426950408889634f;
constexpr float SC2 = 0.08838834764831845f * LOG2E;
constexpr float EPS = 1e-5f;
constexpr int NOUT = 9;

constexpr int WS_EPOCH = 0;
constexpr int WS_QG = 4096;
constexpr int WS_ML = WS_QG + NB * 4 * QN * 8;
constexpr int WS_SG = WS_ML + NWG * 32;
constexpr int SREC = DM * 8;
constexpr int WS_BYTES = WS_SG + NWG * SREC;
constexpr unsigned TAG_STEP = 0x9E3779B1u;
constexpr unsigned SPIN_CAP = 1u << 20;

constexpr int L_XN = 0;
constexpr int WQ_ROW = 40;
constexpr int L_WQ = L_XN + NB * 320;
constexpr int L_QB = L_WQ + 160 * WQ_ROW;
constexpr int L_FLAG = L_QB + 128;
constexpr int L_H = L_FLAG + 16;
constexpr int L_X = L_H + 640;
constexpr int L_MW = L_X + 320;
constexpr int MW_W = 3 * 288;
constexpr int O_ROW = 68;
constexpr int L_O = L_MW + NWAVE * MW_W;
constexpr int L_Y = L_O + 16 * O_ROW;
constexpr int L_ML = L_Y + NWAVE * 16;
constexpr int L_P = L_ML + NWAVE * 4;
constexpr int L_WORDS = L_P + 128;
constexpr int ST_W = 1024;
static_assert((L_WORDS + 2 * NWAVE * ST_W) * 4 <= 163840, "LDS");
static_assert((L_WQ % 4 | L_QB % 4 | L_MW % 4 | L_O % 4 | L_FLAG % 4 | L_P % 4 | L_H % 2 | L_ML % 2) == 0, "align");

typedef unsigned u32x4 __attribute__((ext_vector_type(4)));
typedef unsigned u32x2 __attribute__((ext_vector_type(2)));
typedef unsigned u32x8 __attribute__((ext_vector_type(8)));
typedef __bf16 bf16x16 __attribute__((ext_vector_type(16)));
typedef float f32x4 __attribute__((ext_vector_type(4)));
typedef float f32x2 __attribute__((ext_vector_type(2)));
typedef __bf16 bf16x2 __attribute__((ext_vector_type(2)));
typedef __bf16 bf16x8 __attribute__((ext_vector_type(8)));
typedef short s16x4 __attribute__((ext_vector_type(4)));
#define RLX_AGENT __ATOMIC_RELAXED, __HIP_MEMORY_SCOPE_AGENT
constexpr int AUX_SC1 = 16, AUX_NT = 2, AUX_KV = 18;

__device__ __forceinline__ float dot2(unsigned a, unsigned b, float acc) {
  return __builtin_amdgcn_fdot2_f32_bf16(__builtin_bit_cast(bf16x2, a), __builtin_bit_cast(bf16x2, b), acc, false);
}
__device__ __forceinline__ unsigned cvtpk(float lo, float hi) {
  bf16x2 r = {(__bf16)lo, (__bf16)hi};
  return __builtin_bit_cast(unsigned, r);
}
__device__ __forceinline__ float bf_lo(unsigned v) { return __builtin_bit_cast(float, v << 16); }
__device__ __forceinline__ float bf_hi(unsigned v) { return __builtin_bit_cast(float, v & 0xffff0000u); }
__device__ __forceinline__ float bf2f(unsigned short v) { return __builtin_bit_cast(float, (unsigned)v << 16); }
template <int CTRL> __device__ __forceinline__ float dpp(float x) {
  return __builtin_bit_cast(float, __builtin_amdgcn_mov_dpp(__builtin_bit_cast(int, x), CTRL, 0xf, 0xf, true));
}
constexpr int XOR1 = 0xB1, XOR2 = 0x4E, XOR7 = 0x141, ROR8 = 0x128, ROR4 = 0x124, BCAST = 0x150;
constexpr int QP0 = 0x00, QP1 = 0x55;
__device__ __forceinline__ float ex2(float x) { return __builtin_amdgcn_exp2f(x); }
__device__ __forceinline__ float rcp(float x) { return __builtin_amdgcn_rcpf(x); }

__device__ __forceinline__ float swap32_add(float a, float b) {
  auto s = __builtin_amdgcn_permlane32_swap(__float_as_uint(a), __float_as_uint(b), false, false);
  return __uint_as_float(s[0]) + __uint_as_float(s[1]);
}
__device__ __forceinline__ float swap16_add(float a, float b) {
  auto s = __builtin_amdgcn_permlane16_swap(__float_as_uint(a), __float_as_uint(b), false, false);
  return __uint_as_float(s[0]) + __uint_as_float(s[1]);
}
__device__ __forceinline__ float xrow_max(float x) {
  auto s = __builtin_amdgcn_permlane16_swap(__float_as_uint(x), __float_as_uint(x), false, false);
  x = fmaxf(__uint_as_float(s[0]), __uint_as_float(s[1]));
  auto t = __builtin_amdgcn_permlane32_swap(__float_as_uint(x), __float_as_uint(x), false, false);
  return fmaxf(__uint_as_float(t[0]), __uint_as_float(t[1]));
}
__device__ __forceinline__ float xrow_sum(float x) {
  x = swap16_add(x, x);
  return swap32_add(x, x);
}
__device__ __forceinline__ float row_sum(float x) {
  x += dpp<XOR1>(x);
  x += dpp<XOR2>(x);
  x += dpp<XOR7>(x);
  x += dpp<ROR8>(x);
  return x;
}
__device__ __forceinline__ float wave_sum(float x) { return xrow_sum(row_sum(x)); }
__device__ __forceinline__ float wave_treduce16(float (&v)[16], int lane) {
#pragma unroll
  for (int j = 0; j < 8; ++j) v[j] = swap32_add(v[j], v[j + 8]);
#pragma unroll
  for (int j = 0; j < 4; ++j) v[j] = swap16_add(v[j], v[j + 4]);
#pragma unroll
  for (int j = 0; j < 2; ++j) {
    const float a = v[j] + dpp<ROR8>(v[j]), b = v[j + 2] + dpp<ROR8>(v[j + 2]);
    v[j] = (lane & 8) ? b : a;
  }
  {
    const float a = v[0] + dpp<XOR7>(v[0]), b = v[1] + dpp<XOR7>(v[1]);
    v[0] = (lane & 4) ? b : a;
  }
  float r = v[0];
  r += dpp<XOR2>(r);
  r += dpp<XOR1>(r);
  return r;
}

#define SB() __builtin_amdgcn_sched_barrier(0)
#define PIN8(a) asm volatile("" : "+v"(a[0]), "+v"(a[1]), "+v"(a[2]), "+v"(a[3]), "+v"(a[4]), "+v"(a[5]), "+v"(a[6]), "+v"(a[7]) : : "memory")
#define BARRIER() do { asm volatile("" ::: "memory"); __builtin_amdgcn_s_barrier(); asm volatile("" ::: "memory"); SB(); } while (0)
#define VMW(n) asm volatile("s_waitcnt vmcnt(" STR(n) ")" ::: "memory")
#define LDS_BARRIER() do { asm volatile("s_waitcnt lgkmcnt(0)" ::: "memory"); __builtin_amdgcn_s_barrier(); asm volatile("" ::: "memory"); } while (0)

__global__ void __launch_bounds__(NTHR, 2)
fused_decode_kernel(const unsigned short* __restrict__ x, const unsigned short* __restrict__ kc, const unsigned short* __restrict__ vc,
                    const unsigned short* __restrict__ wq, const unsigned short* __restrict__ wo, const unsigned short* __restrict__ w1,
                    const unsigned short* __restrict__ b1, const unsigned short* __restrict__ w2, const unsigned short* __restrict__ b2,
                    unsigned short* __restrict__ out, unsigned char* ws) {
  __shared__ __attribute__((aligned(16))) unsigned lds[L_WORDS];
  __shared__ __attribute__((aligned(16))) unsigned stg[2 * NWAVE * ST_W];
  float* ldf = reinterpret_cast<float*>(lds);
  const int tid = threadIdx.x, lane = tid & 63, w = __builtin_amdgcn_readfirstlane(tid >> 6);
  const int r = lane >> 4, c = lane & 15;
  unsigned long long pc0;
  unsigned len0;
  asm volatile("s_getpc_b64 %0\n.Lfd_code_begin:\n\ts_mov_b32 %1, .Lfd_code_end-.Lfd_code_begin" : "=s"(pc0), "=s"(len0));
  const unsigned cpf = __builtin_amdgcn_raw_buffer_load_b32(__builtin_amdgcn_make_buffer_rsrc((void*)pc0, 0, len0, 0x00020000), lane * 128, (7 - w) * 8192, 0);
  SB();
  lds[L_FLAG + (tid & 15)] = 0u;
  asm volatile("s_waitcnt lgkmcnt(0)" :: "s"(x), "s"(kc), "s"(vc), "s"(wq), "s"(wo), "s"(w1), "s"(b1), "s"(w2), "s"(b2), "s"(out), "s"(ws) : "memory");
  const int bid = blockIdx.x, xcd = bid & 7, ii = bid >> 3;
  const int kvh = xcd + 8 * ((ii >> 1) & 1), b = (ii >> 2) & 3, sp = (((ii ^ xcd) & 1) << 1) | (ii >> 4);
  const int prod = b * 64 + kvh * 4 + sp;
  const int col0 = (prod & 63) * NOUT;

  unsigned tag = *reinterpret_cast<const __attribute__((address_space(4))) unsigned*>(reinterpret_cast<uintptr_t>(ws + WS_EPOCH));
  const __amdgpu_buffer_rsrc_t rx = __builtin_amdgcn_make_buffer_rsrc((void*)x, 0, NB * DM * 2, 0x00020000);
  const __amdgpu_buffer_rsrc_t rb2 = __builtin_amdgcn_make_buffer_rsrc((void*)b2, 0, 2, 0x00020000);
  const size_t kvbase = (size_t)(b * HKV + kvh) * SK * DH;
  const __amdgpu_buffer_rsrc_t rk = __builtin_amdgcn_make_buffer_rsrc((void*)(kc + kvbase), 0, SK * DH * 2, 0x00020000);
  const __amdgpu_buffer_rsrc_t rv = __builtin_amdgcn_make_buffer_rsrc((void*)(vc + kvbase), 0, SK * DH * 2, 0x00020000);
  const __amdgpu_buffer_rsrc_t rwq = __builtin_amdgcn_make_buffer_rsrc((void*)wq, 0, DM * QN * 2, 0x00020000);
  const __amdgpu_buffer_rsrc_t rwo = __builtin_amdgcn_make_buffer_rsrc((void*)wo, 0, DM * QN * 2, 0x00020000);
  const __amdgpu_buffer_rsrc_t rs = __builtin_amdgcn_make_buffer_rsrc((void*)ws, 0, WS_BYTES, 0x00020000);
  const int lo16 = lane * 16;
  const int wpo = ((w & 3) << 12) | ((sp >> 1) << 14) | ((w >> 2) << 18) | ((sp & 1) << 19);
  constexpr int HALF = 1 << 15;
#define POFF(p) (wpo + (((p) & 3) << 16) + (((p) >> 2) << 20))
#define COFF(n) (POFF((n) >> 1) + ((n) & 1) * HALF)
#define LDC(rs_, n, t) __builtin_amdgcn_raw_buffer_load_b128(rs_, lo16 + (t) * 1024, COFF(n), AUX_KV)
  u32x4 cb0[4], cb1[4], cb2[4], cb3[4], cb4[4], cb5[4], cb6[4], cb7[4];
  const int cb = bid & 63, ks = bid >> 6;
  unsigned xr[5], xn[5];
  u32x4 wqr[5];
#define LDK(dst, n) do { SB(); _Pragma("unroll") for (int t = 0; t < 4; ++t) { dst[t] = LDC(rk, n, t); SB(); } } while (0)
  const int oob = lane < 32 ? 0 : 0x7fff0000;
  const int hd = w < 4 ? 0 : 0x10000000;
  const int wqv = (lane >> 3) * QN * 2 + (lane & 7) * 16;
  const int wqs = ((ks * 144 + w * 36) * QN + cb * 64) * 2 + hd;
#pragma unroll
  for (int j = 0; j < 5; ++j) xr[j] = __builtin_amdgcn_raw_buffer_load_b32(rx, lane * 4 + j * 256 + (j == 4 ? oob : 0), w * DM * 2 + hd, 0);
  SB();
#pragma unroll
  for (int j = 0; j < 5; ++j) wqr[j] = __builtin_amdgcn_raw_buffer_load_b128(rwq, wqv + (j == 4 ? oob : 0), wqs + j * 8 * QN * 2, AUX_NT);
  SB();
  BARRIER();
  LDK(cb0, 0);
  if (w < 4) {
    int xo_[5];
#pragma unroll
    for (int k = 0; k < 5; ++k) {
      const int G = 4 * k + r, blk = (G * 13) >> 6;
      xo_[k] = (L_XN + (c & 3) * 320 + ks * 72 + blk * 18 + (G - 5 * blk) * 4) * 4;
      asm volatile("" : "+v"(xo_[k]));
    }
    int wqa_ = ((8 * r + (c >> 2)) * WQ_ROW + w * 8 + (c & 3) * 2) * 4;
    int qso_ = c < 8 ? (c & 3) * 4 * QN * 8 + r * 32 + (c >> 2) * 16 : 0x7fffffff;
    asm volatile("" : "+v"(wqa_), "+v"(qso_));
    SB();
    float xv[10];
    float s = 0.f;
#pragma unroll
    for (int j = 0; j < 5; ++j) { xv[2 * j] = bf_lo(xr[j]); xv[2 * j + 1] = bf_hi(xr[j]); s += xv[2 * j] + xv[2 * j + 1]; }
    const float mean = wave_sum(s) * (1.0f / DM);
    float v = 0.f;
#pragma unroll
    for (int j = 0; j < 10; ++j) { xv[j] = (j < 8 || lane < 32) ? xv[j] - mean : 0.f; v = fmaf(xv[j], xv[j], v); }
    const float rstd = __builtin_amdgcn_rsqf(wave_sum(v) * (1.0f / DM) + EPS);
#pragma unroll
    for (int j = 0; j < 5; ++j) { xn[j] = cvtpk(xv[2 * j] * rstd, xv[2 * j + 1] * rstd); lds[L_XN + w * 320 + j * 64 + lane] = xn[j]; }
    SB();
#pragma unroll
    for (int j = 0; j < 5; ++j) *reinterpret_cast<u32x4*>(lds + L_WQ + (w * 40 + j * 8 + (lane >> 3)) * WQ_ROW + (lane & 7) * 4) = wqr[j];
    asm volatile("" ::: "memory");
    if (lane == 0) __hip_atomic_fetch_add(lds + L_FLAG + 5, 1u, __ATOMIC_RELAXED, __HIP_MEMORY_SCOPE_WORKGROUP);
    while (__builtin_amdgcn_readfirstlane(__hip_atomic_load(lds + L_FLAG + 5, __ATOMIC_RELAXED, __HIP_MEMORY_SCOPE_WORKGROUP)) != 4u) {}
    asm volatile("" ::: "memory");
    typedef __attribute__((address_space(3))) s16x4* lds_s16x4;
    const unsigned* wqa = reinterpret_cast<const unsigned*>(reinterpret_cast<const char*>(lds + L_WQ) + wqa_);
    f32x4 acc0 = {0.f, 0.f, 0.f, 0.f}, acc1 = {0.f, 0.f, 0.f, 0.f};
    u32x4 af[5], bf[5];
#pragma unroll
    for (int k = 0; k < 5; ++k) {
      const u32x2 lo = __builtin_bit_cast(u32x2, __builtin_amdgcn_ds_read_tr16_b64_v4i16((lds_s16x4)(wqa + (32 * k) * WQ_ROW)));
      const u32x2 hi = __builtin_bit_cast(u32x2, __builtin_amdgcn_ds_read_tr16_b64_v4i16((lds_s16x4)(wqa + (32 * k + 4) * WQ_ROW)));
      af[k] = u32x4{lo[0], lo[1], hi[0], hi[1]};
      const unsigned* xp = reinterpret_cast<const unsigned*>(reinterpret_cast<const char*>(lds) + xo_[k]);
      const u32x2 x0 = *reinterpret_cast<const u32x2*>(xp), x1 = *reinterpret_cast<const u32x2*>(xp + 2);
      bf[k] = u32x4{x0[0], x0[1], x1[0], x1[1]};
    }
    SB();
#pragma unroll
    for (int k = 0; k < 5; ++k) {
      if (k & 1) acc1 = __builtin_amdgcn_mfma_f32_16x16x32_bf16(__builtin_bit_cast(bf16x8, af[k]), __builtin_bit_cast(bf16x8, bf[k]), acc1, 0, 0, 0);
      else acc0 = __builtin_amdgcn_mfma_f32_16x16x32_bf16(__builtin_bit_cast(bf16x8, af[k]), __builtin_bit_cast(bf16x8, bf[k]), acc0, 0, 0, 0);
    }
    unsigned tg;
    asm volatile("s_add_u32 %0, %1, %2" : "=s"(tg) : "s"(tag), "i"(TAG_STEP) : "scc");
    const float e0 = (c & 4) ? acc0[2] + acc1[2] : acc0[0] + acc1[0], e1 = (c & 4) ? acc0[3] + acc1[3] : acc0[1] + acc1[1];
    const u32x4 gq = {tg, __float_as_uint(e0), tg, __float_as_uint(e1)};
    __builtin_amdgcn_raw_buffer_store_b128(gq, rs, qso_, WS_QG + (ks * QN + cb * 64 + w * 16) * 8, AUX_KV);
  } else {
    VMW(0);
  }
  LDK(cb1, 1);
  asm volatile("s_add_u32 %0, %0, %1" : "+s"(tag) : "i"(TAG_STEP) : "scc");
  if (w == b) {
    float xv[10];
    float s2 = 0.f;
#pragma unroll
    for (int j = 0; j < 5; ++j) { xv[2 * j] = bf_lo(xn[j]); xv[2 * j + 1] = bf_hi(xn[j]); s2 += xv[2 * j] + xv[2 * j + 1]; lds[L_X + j * 64 + lane] = xr[j]; }
    const float mean2 = wave_sum(s2) * (1.0f / DM);
    float v2 = 0.f;
#pragma unroll
    for (int j = 0; j < 10; ++j) { xv[j] = (j < 8 || lane < 32) ? xv[j] - mean2 : 0.f; v2 = fmaf(xv[j], xv[j], v2); }
    const float rstd2 = __builtin_amdgcn_rsqf(wave_sum(v2) * (1.0f / DM) + EPS);
#pragma unroll
    for (int j = 0; j < 5; ++j) *reinterpret_cast<float2*>(ldf + L_H + j * 128 + lane * 2) = make_float2(xv[2 * j] * rstd2, xv[2 * j + 1] * rstd2);
  }
  const int qo = WS_QG + ((b * 4 + (lane >> 5) * 2) * QN + kvh * 256 + w * 64 + (lane & 31) * 2) * 8;
  bool need = w < 4;
  auto q_try = [&](const u32x4& g0, const u32x4& g1) {
    const bool ok = (g0[0] == tag) & (g0[2] == tag) & (g1[0] == tag) & (g1[2] == tag);
    if (__all(ok)) {
      const float q0 = __uint_as_float(g0[1]) + __uint_as_float(g1[1]), q1 = __uint_as_float(g0[3]) + __uint_as_float(g1[3]);
      const float qa = swap32_add(q0, q0), qb = swap32_add(q1, q1);
      if (lane < 32) lds[L_QB + w * 32 + lane] = cvtpk(qa, qb);
      if (lane == 0) lds[L_FLAG + 8] = tag;
      asm volatile("" ::: "memory");
      if (lane == 0) __hip_atomic_store(lds + L_FLAG + w, 1u, __ATOMIC_RELAXED, __HIP_MEMORY_SCOPE_WORKGROUP);
      asm volatile("" ::: "memory");
      need = false;
    }
  };
  auto q_ready = [&]() {
    const unsigned long long f = __hip_atomic_load(reinterpret_cast<unsigned long long*>(lds + L_FLAG), __ATOMIC_RELAXED, __HIP_MEMORY_SCOPE_WORKGROUP) &
                                 __hip_atomic_load(reinterpret_cast<unsigned long long*>(lds + L_FLAG + 2), __ATOMIC_RELAXED, __HIP_MEMORY_SCOPE_WORKGROUP);
    return __builtin_amdgcn_readfirstlane((unsigned)f & (unsigned)(f >> 32)) != 0;
  };
  unsigned pf0 = 0, pf1 = 0;
#define CHUNK(dst, n) do { SB(); if (w < 4 && (n) <= 2) VMW(4); else VMW(0); SB(); \
    if ((n) == 6 && w >= 4) { \
      const int po_ = (kvh * 256 + (b * 4 + sp) * 16 + (w - 4) * 4) * (DM * 2), pv_ = lane < 36 ? lane * 128 : 0x7fffffff; \
      pf0 = __builtin_amdgcn_raw_buffer_load_b32(rwo, pv_, po_, AUX_KV); SB(); \
      pf1 = __builtin_amdgcn_raw_buffer_load_b32(rwo, pv_, po_, AUX_SC1); SB(); } \
    if ((n) >= 6) { \
    const int qs_ = need ? 0 : 0x10000000; \
    const u32x4 g0_ = __builtin_amdgcn_raw_buffer_load_b128(rs, qo, qs_, AUX_KV), g1_ = __builtin_amdgcn_raw_buffer_load_b128(rs, qo, qs_ + QN * 8, AUX_KV); \
    LDK(dst, n); \
    if (need) q_try(g0_, g1_); \
    } else LDK(dst, n); \
    SB(); } while (0)
  CHUNK(cb2, 2);
  CHUNK(cb3, 3);
  CHUNK(cb4, 4);
  CHUNK(cb5, 5);
  CHUNK(cb6, 6);
  CHUNK(cb7, 7);
#pragma unroll 1
  for (unsigned spins = 0; need && spins < SPIN_CAP; ++spins) {
    const u32x4 g0_ = __builtin_amdgcn_raw_buffer_load_b128(rs, qo, 0, AUX_KV), g1_ = __builtin_amdgcn_raw_buffer_load_b128(rs, qo, QN * 8, AUX_KV);
    q_try(g0_, g1_);
  }
  if (need && lane == 0) __hip_atomic_store(lds + L_FLAG + w, 1u, __ATOMIC_RELAXED, __HIP_MEMORY_SCOPE_WORKGROUP);
  u32x4 mw[4];
  const unsigned short b2r = __builtin_amdgcn_raw_buffer_load_b16(rb2, 0, 0, 0);
  {
    const __amdgpu_buffer_rsrc_t r1 = __builtin_amdgcn_make_buffer_rsrc((void*)w1, 0, FF * 2, 0x00020000);
    const __amdgpu_buffer_rsrc_t r2 = __builtin_amdgcn_make_buffer_rsrc((void*)b1, 0, FF * 2, 0x00020000);
    const __amdgpu_buffer_rsrc_t r3 = __builtin_amdgcn_make_buffer_rsrc((void*)w2, 0, FF * 2, 0x00020000);
    const int mo = w * 1152;
    mw[0] = __builtin_amdgcn_raw_buffer_load_b128(r1, lo16, mo, 0);
    mw[1] = __builtin_amdgcn_raw_buffer_load_b128(r2, lo16, mo, 0);
    mw[2] = __builtin_amdgcn_raw_buffer_load_b128(r3, lo16, mo, 0);
    const int l4 = lane < 32 ? lane * 4 : 0x7fffffff;
    mw[3][0] = __builtin_amdgcn_raw_buffer_load_b32(r1, l4, mo + 1024, 0);
    mw[3][1] = __builtin_amdgcn_raw_buffer_load_b32(r2, l4, mo + 1024, 0);
    mw[3][2] = __builtin_amdgcn_raw_buffer_load_b32(r3, l4, mo + 1024, 0);
  }
  SB();
  while (!q_ready()) __builtin_amdgcn_s_sleep(1);
  asm volatile("" ::: "memory");
  asm volatile("" :: "v"(cpf), "v"(pf0), "v"(pf1));
  tag = __builtin_amdgcn_readfirstlane(lds[L_FLAG + 8]);
  const u32x4 qh0 = *reinterpret_cast<const u32x4*>(lds + L_QB + c * 4);
  const u32x4 qh1 = *reinterpret_cast<const u32x4*>(lds + L_QB + 64 + c * 4);
  float hv[NOUT];
  float ya[16];
#pragma unroll
  for (int j = 0; j < 16; ++j) ya[j] = 0.f;
#pragma unroll
  for (int j = 0; j < NOUT; ++j) hv[j] = __builtin_bit_cast(float, __builtin_amdgcn_readfirstlane(lds[L_H + col0 + j]));
  const unsigned short* mwl = reinterpret_cast<const unsigned short*>(lds + L_MW + w * MW_W);

  auto mlp_slice = [&](int k) {
    const int f = k * 64 + lane;
    const float w1f = bf2f(mwl[f]), b1f = bf2f(mwl[576 + f]), w2f = bf2f(mwl[2 * 576 + f]);
    constexpr float C1 = -2.0f * 0.7978845608028654f * LOG2E, C2 = C1 * 0.044715f;
#pragma unroll
    for (int j = 0; j < NOUT; ++j) {
      const float uu = fmaf(hv[j], w1f, b1f);
      const float e = ex2(uu * fmaf(uu * uu, C2, C1));
      ya[j] = fmaf(uu * w2f, rcp(1.0f + e), ya[j]);
    }
  };

#define PINY() asm volatile("" : "+v"(ya[0]), "+v"(ya[1]), "+v"(ya[2]), "+v"(ya[3]), "+v"(ya[4]), "+v"(ya[5]), "+v"(ya[6]), "+v"(ya[7]), "+v"(ya[8]))
#define MLP(k) do { mlp_slice(k); PINY(); SB(); } while (0)
#define ISSUE(dst, rs_, so) do { VMW(0); SB(); _Pragma("unroll") for (int t = 0; t < 4; ++t) { \
      dst[t] = __builtin_amdgcn_raw_buffer_load_b128(rs_, lo16 + t * 1024, so, AUX_KV); SB(); } } while (0)
#define NOISSUE(dst, rs_, so) do {} while (0)
  typedef __attribute__((address_space(3))) void* lds_ptr;
  typedef __attribute__((address_space(3))) s16x4* lds_tr;
  u32x8 wf[5][4];
  const int wv = (lane >> 3) * (DM * 2) + (lane & 7) * 16;
  const int wso = kvh * 256 * (DM * 2) + w * 128, wso5 = (kvh * 256 + w * 32) * (DM * 2) + 1024;
  const int tro = (8 * r + (c >> 2)) * 128 + (c & 3) * 8;
  const char* tra = reinterpret_cast<const char*>(stg + w * ST_W) + tro;
  const char* tr5 = reinterpret_cast<const char*>(stg + NWAVE * ST_W) + tro + (w & 3) * 32;
#define WO_ISSUE(j) do { _Pragma("unroll") for (int g = 0; g < 4; ++g) { \
      __builtin_amdgcn_raw_ptr_buffer_load_lds(rwo, (lds_ptr)(stg + w * ST_W + g * 256), 16, wv, wso + (32 * (j) + 8 * g) * (DM * 2), 0, 0); SB(); } \
    if ((j) < 4) __builtin_amdgcn_raw_ptr_buffer_load_lds(rwo, (lds_ptr)(stg + (NWAVE + w) * ST_W + (j) * 256), 16, wv, wso5 + 8 * (j) * (DM * 2), 0, 0); \
    SB(); } while (0)
#define TR_READ(t, j, p) _Pragma("unroll") for (int u = 0; u < 2; ++u) { \
      const u32x2 v_ = __builtin_bit_cast(u32x2, __builtin_amdgcn_ds_read_tr16_b64_v4i16((lds_tr)((p) + u * 512))); \
      wf[t][(j) >> 1][((j) & 1) * 4 + 2 * u] = v_[0]; wf[t][(j) >> 1][((j) & 1) * 4 + 2 * u + 1] = v_[1]; }
#define WO_READ(j) do { _Pragma("unroll") for (int t = 0; t < 4; ++t) { TR_READ(t, j, tra + t * 32) } \
    if ((j) == 3 && lane == 0) __hip_atomic_fetch_add(lds + L_FLAG + 9, 1u, __ATOMIC_RELAXED, __HIP_MEMORY_SCOPE_WORKGROUP); \
    SB(); } while (0)
#define LOADS(dst, rs_, so) do { _Pragma("unroll") for (int t = 0; t < 4; ++t) { \
      dst[t] = __builtin_amdgcn_raw_buffer_load_b128(rs_, lo16 + t * 1024, so, AUX_KV); SB(); } } while (0)
#define ALLW() asm volatile("s_waitcnt vmcnt(0) lgkmcnt(0)" ::: "memory")
#define ISSUE_W0(dst, rs_, so) do { ALLW(); SB(); WO_ISSUE(0); LOADS(dst, rs_, so); } while (0)
#define ISSUE_R0(dst, rs_, so) do { VMW(0); SB(); LOADS(dst, rs_, so); WO_READ(0); } while (0)
#define ISSUE_W1(dst, rs_, so) do { ALLW(); SB(); WO_ISSUE(1); LOADS(dst, rs_, so); } while (0)
#define ISSUE_R1(dst, rs_, so) do { VMW(0); SB(); LOADS(dst, rs_, so); WO_READ(1); } while (0)
#define QK_HALF(kb, hf) _Pragma("unroll") for (int t = 0; t < 4; ++t) { \
      float d0 = 0.f, d1 = 0.f; \
      _Pragma("unroll") for (int k = 0; k < 4; ++k) { d0 = dot2(kb[t][k], qh0[k], d0); d1 = dot2(kb[t][k], qh1[k], d1); } \
      xs[8 * (hf) + 2 * t] = d0; xs[8 * (hf) + 2 * t + 1] = d1; }
#define TR_STEP(HF, CTRL, BIT) _Pragma("unroll") for (int h = 0; h < (HF); ++h) { \
      const float t1_ = xs[h] + dpp<CTRL>(xs[h]), t2_ = xs[h + (HF)] + dpp<CTRL>(xs[h + (HF)]); \
      xs[h] = (c & (BIT)) ? t2_ : t1_; }
#define K_PAGE2(pg, blo, bhi, ISS, nlo, rs_, so, ISS2, nhi, rs2_, so2) do { \
    float xs[16]; \
    QK_HALF(blo, 0) \
    PIN8(xs); SB(); \
    ISS(nlo, rs_, so); \
    QK_HALF(bhi, 1) \
    PIN8((xs + 8)); SB(); \
    ISS2(nhi, rs2_, so2); \
    TR_STEP(8, ROR8, 8) TR_STEP(4, XOR7, 4) TR_STEP(2, XOR2, 2) TR_STEP(1, XOR1, 1) \
    sc[pg] = xs[0]; \
    asm volatile("" : "+v"(sc[pg])); SB(); \
  } while (0)
#define PV_STEP(vb, t, hf, p) { const float p0 = dpp<BCAST + 8 * (hf) + 2 * (t)>(p), p1 = dpp<BCAST + 8 * (hf) + 2 * (t) + 1>(p); \
      _Pragma("unroll") for (int k = 0; k < 4; ++k) { const f32x2 v_ = {bf_lo(vb[t][k]), bf_hi(vb[t][k])}; \
        a0[k] = __builtin_elementwise_fma(f32x2{p0, p0}, v_, a0[k]); a1[k] = __builtin_elementwise_fma(f32x2{p1, p1}, v_, a1[k]); } }
#define PINA() asm volatile("" : "+v"(a0[0]), "+v"(a0[1]), "+v"(a0[2]), "+v"(a0[3]), "+v"(a1[0]), "+v"(a1[1]), "+v"(a1[2]), "+v"(a1[3]) : : "memory")
#define PV_HALF(vb, hf, p) do { PV_STEP(vb, 0, hf, p) PV_STEP(vb, 1, hf, p) PV_STEP(vb, 2, hf, p) PV_STEP(vb, 3, hf, p) PINA(); SB(); } while (0)
  float sc[8];
  K_PAGE2(0, cb0, cb1, ISSUE, cb0, rk, COFF(8), NOISSUE, cb0, rk, 0);
  K_PAGE2(1, cb2, cb3, ISSUE, cb1, rk, COFF(9), NOISSUE, cb0, rk, 0);
  {
    unsigned* mwd = lds + L_MW + w * MW_W;
#pragma unroll
    for (int j = 0; j < 3; ++j) *reinterpret_cast<u32x4*>(mwd + j * 288 + lane * 4) = mw[j];
    if (lane < 32) {
#pragma unroll
      for (int j = 0; j < 3; ++j) mwd[j * 288 + 256 + lane] = mw[3][j];
    }
    if (tid == 0) ldf[L_FLAG + 4] = bf2f(b2r);
    asm volatile("" ::: "memory");
  }
  K_PAGE2(2, cb4, cb5, ISSUE, cb2, rk, COFF(10), NOISSUE, cb0, rk, 0);
  MLP(0);
  K_PAGE2(3, cb6, cb7, ISSUE, cb3, rk, COFF(11), NOISSUE, cb0, rk, 0);
  MLP(1);
  K_PAGE2(4, cb0, cb1, ISSUE, cb4, rk, COFF(12), NOISSUE, cb0, rk, 0);
  K_PAGE2(5, cb2, cb3, ISSUE, cb5, rk, COFF(13), NOISSUE, cb0, rk, 0);
  MLP(2);
  K_PAGE2(6, cb4, cb5, ISSUE_W0, cb2, rk, COFF(14), ISSUE_R0, cb3, rk, COFF(15));
  MLP(3);
  K_PAGE2(7, cb2, cb3, ISSUE_W1, cb0, rv, COFF(0), ISSUE_R1, cb1, rv, COFF(1));
  float m = fmaxf(fmaxf(fmaxf(sc[0], sc[1]), fmaxf(sc[2], sc[3])), fmaxf(fmaxf(sc[4], sc[5]), fmaxf(sc[6], sc[7])));
  m = fmaxf(m, dpp<XOR2>(m));
  m = fmaxf(m, dpp<ROR4>(m));
  m = fmaxf(m, dpp<ROR8>(m));
  m *= SC2;
  float l = 0.f;
#pragma unroll
  for (int k = 0; k < 8; ++k) { sc[k] = ex2(fmaf(sc[k], SC2, -m)); l += sc[k]; }
  f32x2 a0[4], a1[4];
#pragma unroll
  for (int k = 0; k < 4; ++k) { a0[k] = f32x2{0.f, 0.f}; a1[k] = f32x2{0.f, 0.f}; }
  {
    const float mw_ = xrow_max(m);
    float lr = l * ex2(m - mw_);
    lr += dpp<XOR2>(lr);
    lr += dpp<ROR4>(lr);
    lr += dpp<ROR8>(lr);
    lr = xrow_sum(lr);
    if (lane < 2) *reinterpret_cast<float2*>(ldf + L_ML + w * 4 + lane * 2) = make_float2(mw_, lr);
    asm volatile("" ::: "memory");
    unsigned cnt = 0;
    if (lane == 0) cnt = __hip_atomic_fetch_add(lds + L_FLAG + 6, 1u, __ATOMIC_RELAXED, __HIP_MEMORY_SCOPE_WORKGROUP);
    asm volatile("" ::: "memory");
    const bool last_ = __builtin_amdgcn_readfirstlane(cnt) == NWAVE - 1;
    {
      const float2 ml = *reinterpret_cast<const float2*>(ldf + L_ML + c * 2);
      float M = fmaxf(ml.x, dpp<XOR2>(ml.x));
      M = fmaxf(M, dpp<ROR4>(M));
      M = fmaxf(M, dpp<ROR8>(M));
      float L = ml.y * ex2(ml.x - M);
      L += dpp<XOR2>(L);
      L += dpp<ROR4>(L);
      L += dpp<ROR8>(L);
      const u32x4 gm = {tag, __float_as_uint(M), tag, __float_as_uint(L)};
      __builtin_amdgcn_raw_buffer_store_b128(gm, rs, lane < 2 ? lo16 : 0x7fffffff, last_ ? WS_ML + prod * 32 : 0x10000000, AUX_SC1);
    }
    SB();
  }
  bool need2 = w == 6;
  auto ml_try = [&](const u32x4& g) {
    if (__all((g[0] == tag) & (g[2] == tag))) {
      const float Ms = __uint_as_float(g[1]);
      float M = fmaxf(Ms, dpp<XOR2>(Ms));
      M = fmaxf(M, dpp<ROR4>(M));
      float L = __uint_as_float(g[3]) * ex2(Ms - M);
      L += dpp<XOR2>(L);
      L += dpp<ROR4>(L);
      if (lane < 2) *reinterpret_cast<float2*>(ldf + L_FLAG + 10 + lane * 2) = make_float2(M, 1.0f / L);
      asm volatile("" ::: "memory");
      if (lane == 0) __hip_atomic_store(lds + L_FLAG + 7, 1u, __ATOMIC_RELAXED, __HIP_MEMORY_SCOPE_WORKGROUP);
      asm volatile("" ::: "memory");
      need2 = false;
    }
  };
  const int mlo = (lane & 7) * 16, mls = WS_ML + (prod & ~3) * 32;
  u32x4 gp_;
#define V_PAGE(pg, j, POLL) do { \
    PV_HALF(cb0, 0, sc[pg]); \
    if ((pg) == 0) asm volatile("s_waitcnt vmcnt(1) lgkmcnt(0)" ::: "memory"); else ALLW(); \
    SB(); \
    if ((j) < 8) WO_ISSUE(j); \
    LOADS(cb0, rv, COFF(2 * (pg) + 2)); \
    if ((pg) >= 4) { if (need2) ml_try(gp_); SB(); } \
    PV_HALF(cb1, 1, sc[pg]); \
    if ((pg) == 6) VMW(4); else VMW(0);                      \
    SB(); \
    if (POLL) { \
      gp_ = __builtin_amdgcn_raw_buffer_load_b128(rs, mlo, need2 ? mls : 0x10000000, AUX_SC1); SB(); \
      LOADS(cb1, rv, COFF(2 * (pg) + 3)); \
      if ((j) < 8) WO_READ(j); \
      if ((pg) == 6 && need2) ml_try(gp_); \
      SB(); \
    } else { \
      LOADS(cb1, rv, COFF(2 * (pg) + 3)); \
      if ((j) < 8) WO_READ(j); \
    } } while (0)
  MLP(4);
  V_PAGE(0, 2, 0);
  MLP(5);
  V_PAGE(1, 3, 0);
  MLP(6);
  V_PAGE(2, 4, 0);
  MLP(7);
  V_PAGE(3, 5, 1);
  MLP(8);
  {
    const float ysum = wave_treduce16(ya, lane);
    if ((lane & 3) == 0) ldf[L_Y + w * 16 + (lane >> 2)] = ysum;
    SB();
  }
  V_PAGE(4, 6, 1);
  V_PAGE(5, 7, 1);
  V_PAGE(6, 8, 1);
#pragma unroll 1
  for (unsigned spins = 0; need2 && spins < SPIN_CAP; ++spins) ml_try(__builtin_amdgcn_raw_buffer_load_b128(rs, mlo, mls, AUX_SC1));
  if (need2 && lane == 0) __hip_atomic_store(lds + L_FLAG + 7, 1u, __ATOMIC_RELAXED, __HIP_MEMORY_SCOPE_WORKGROUP);
  while (__builtin_amdgcn_readfirstlane(__hip_atomic_load(lds + L_FLAG + 7, __ATOMIC_RELAXED, __HIP_MEMORY_SCOPE_WORKGROUP)) == 0) __builtin_amdgcn_s_sleep(1);
  asm volatile("" ::: "memory");
  {
    const float2 g = *reinterpret_cast<const float2*>(ldf + L_FLAG + 10 + (c & 1) * 2);
    const float f = ex2(m - g.x) * g.y;
    const float s0 = dpp<QP0>(f), s1 = dpp<QP1>(f);
#pragma unroll
    for (int k = 0; k < 4; ++k) { a0[k] *= s0; a1[k] *= s1; }
    sc[7] *= f;
  }
  while (__builtin_amdgcn_readfirstlane(__hip_atomic_load(lds + L_FLAG + 9, __ATOMIC_RELAXED, __HIP_MEMORY_SCOPE_WORKGROUP)) != NWAVE) __builtin_amdgcn_s_sleep(1);
  asm volatile("" ::: "memory");
#pragma unroll
  for (int j = 0; j < 8; ++j) { TR_READ(4, j, tr5 + j * 4096) }
  int oa_ = (L_O + w * 2 * O_ROW + 32 * (r >> 1) + 2 * c + (r & 1)) * 4, ra_ = (L_O + c * O_ROW + 4 * r) * 4;
  const int so5 = ((512 + 16 * w + c - 1) * 8) | ((w >> 2 | r) << 28);
  int so_ = (c & 1) ? so5 : (64 * w + lane) * 8;
  int idx_ = (c & 1) ? 0xEEEE : 0x4444;
  asm volatile("" : "+v"(sc[7]), "+v"(oa_), "+v"(ra_), "+v"(so_), "+v"(idx_));
  f32x4 acc[5];
#pragma unroll
  for (int t = 0; t < 5; ++t) acc[t] = f32x4{0.f, 0.f, 0.f, 0.f};
  u32x4 gs = {tag, 0u, tag, 0u};
  int sso_ = WS_SG + prod * SREC;
  asm volatile("" : "+v"(acc[0]), "+v"(acc[1]), "+v"(acc[2]), "+v"(acc[3]), "+v"(acc[4]), "+v"(gs));
  asm volatile("" : "+s"(sso_));
  PINA();
  SB();
  PV_HALF(cb0, 0, sc[7]);
  PV_HALF(cb1, 1, sc[7]);
  {
    float t8[8];
#pragma unroll
    for (int k = 0; k < 8; ++k) t8[k] = swap32_add(a0[k >> 1][k & 1], a1[k >> 1][k & 1]);
    float t4[4];
#pragma unroll
    for (int k = 0; k < 4; ++k) t4[k] = swap16_add(t8[k], t8[k + 4]);
    unsigned* rec = reinterpret_cast<unsigned*>(reinterpret_cast<char*>(lds) + oa_);
    rec[0] = cvtpk(t4[0], t4[1]);
    rec[O_ROW] = cvtpk(t4[2], t4[3]);
  }
  LDS_BARRIER();
  {
    u32x4 af[4];
#pragma unroll
    for (int s = 0; s < 4; ++s) af[s] = *reinterpret_cast<const u32x4*>(reinterpret_cast<const char*>(lds) + ra_ + s * 64);
    SB();
#define TILE(t) _Pragma("unroll") for (int s = 0; s < 4; ++s) \
      acc[t] = __builtin_amdgcn_smfmac_f32_16x16x64_bf16(__builtin_bit_cast(bf16x8, af[s]), __builtin_bit_cast(bf16x16, wf[t][s]), acc[t], idx_, 0, 0)
#define RSUM(t) ((acc[t][0] + acc[t][1]) + (acc[t][2] + acc[t][3]))
    if (w < 4) { TILE(4); }
    SB();
    TILE(0);
    SB();
    TILE(2);
    float x5 = xrow_sum(RSUM(4));
    float xn5 = dpp<XOR1>(x5);
    asm volatile("" : "+v"(x5), "+v"(xn5));
    SB();
    TILE(1);
    float d0 = RSUM(0);
    asm volatile("" : "+v"(d0));
    SB();
    TILE(3);
    float d02 = swap32_add(d0, RSUM(2));
    float d1 = RSUM(1);
    asm volatile("" : "+v"(d02), "+v"(d1));
    SB();
    const float v = swap16_add(d02, swap32_add(d1, RSUM(3)));
    const float vn = dpp<XOR1>(v);
    gs[1] = __float_as_uint((c & 1) ? xn5 : v);
    gs[3] = __float_as_uint((c & 1) ? x5 : vn);
    __builtin_amdgcn_raw_buffer_store_b128(gs, rs, so_, sso_, AUX_SC1);
  }
  {
    const int odd = prod & 1, pr = lane & 7;
    const int so = WS_SG + (b * 64 + w * 8) * SREC + (col0 - odd) * 8;
    const int vo = (lane >> 3) * SREC + pr * 16;
    const unsigned long long m5 = 0x1f1f1f1f1f1f1f1full;
    u32x4 ga;
    asm volatile("s_mov_b64 exec, %4\n\tbuffer_load_dwordx4 %0, %1, %2, %3 offen sc1\n\ts_mov_b64 exec, -1" : "={v[240:243]}"(ga) : "v"(vo), "s"(rs), "s"(so), "s"(m5) : "memory");
    SB();
    const int jo = 2 * pr - odd + ((lane >> 3) & 1);
    const int jj = min(max(jo, 0), NOUT - 1);
    float y = ldf[L_FLAG + 4];
#pragma unroll
    for (int ww = 0; ww < NWAVE; ++ww) y += ldf[L_Y + ww * 16 + jj];
    float g = y / (1.0f + __expf(-y));
    float xo = bf2f(reinterpret_cast<const unsigned short*>(lds + L_X)[col0 + jj]);
    const unsigned lb = (unsigned)(uintptr_t)(__attribute__((address_space(3))) unsigned*)lds;
    unsigned pa_ = lb + (L_P + c * 8 + w) * 4, pb_ = lb + (L_P + c * 8) * 4, pc_ = lb + (L_FLAG + 15) * 4, one_ = 1;
    int ovo = lane < 16 && pr < 5 && jo >= 0 && jo < NOUT ? (b * DM + col0 + jo) * 2 : 0x7fffffff;
    const __amdgpu_buffer_rsrc_t ro = __builtin_amdgcn_make_buffer_rsrc((void*)out, 0, NB * DM * 2, 0x00020000);
    asm volatile("" : "+v"(g), "+v"(xo), "+v"(pa_), "+v"(pb_), "+v"(pc_), "+v"(one_), "+v"(ovo));
    asm volatile("" :: "s"(ro));
    SB();
    float e0 = 0.f, e1 = 0.f;
    {
      unsigned long long tm;
      unsigned spins = SPIN_CAP;
      asm volatile(
        "s_mov_b64 exec, %9\n"
        ".Lfd_poll:\n\t"
        "s_waitcnt vmcnt(0)\n\t"
        "v_cmp_eq_u32 vcc, %8, v240\n\t"
        "v_cmp_eq_u32 %2, %8, v242\n\t"
        "s_and_b64 vcc, vcc, %2\n\t"
        "v_cndmask_b32 %0, %0, v241, vcc\n\t"
        "v_cndmask_b32 %1, %1, v243, vcc\n\t"
        "s_andn2_b64 exec, exec, vcc\n\t"
        "s_cbranch_execz .Lfd_polled\n\t"
        "s_sub_u32 %3, %3, 1\n\t"
        "s_cbranch_scc1 .Lfd_polled\n\t"
        "buffer_load_dwordx4 v[240:243], %5, %6, %7 offen sc1\n\t"
        "s_branch .Lfd_poll\n"
        ".Lfd_polled:\n\t"
        "s_mov_b64 exec, -1"
        : "+v"(e0), "+v"(e1), "=&s"(tm), "+s"(spins), "+{v[240:243]}"(ga)
        : "v"(vo), "s"(rs), "s"(so), "s"(tag), "s"(m5) : "vcc", "scc", "memory");
    }
    const float u0 = e0 + dpp<ROR8>(e0), u1 = e1 + dpp<ROR8>(e1);
    const float s = xrow_sum((lane & 8) ? u1 : u0);
    unsigned cnt;
    f32x4 p0, p1;
    asm volatile(
      "ds_write_b32 %3, %4\n\t"
      "s_mov_b64 exec, 1\n\t"
      "ds_add_rtn_u32 %0, %5, %6\n\t"
      "s_mov_b64 exec, -1\n\t"
      "ds_read_b128 %1, %7\n\t"
      "ds_read_b128 %2, %7 offset:16\n\t"
      "s_waitcnt lgkmcnt(0)"
      : "=&v"(cnt), "=&v"(p0), "=&v"(p1) : "v"(pa_), "v"(s), "v"(pc_), "v"(one_), "v"(pb_) : "memory");
    if (__builtin_amdgcn_readfirstlane(cnt) == NWAVE - 1) {
      const f32x4 q4 = p0 + p1;
      const f32x2 q2 = f32x2{q4[0], q4[1]} + f32x2{q4[2], q4[3]};
      __builtin_amdgcn_raw_buffer_store_b16((unsigned short)(cvtpk(fmaf(q2[0] + q2[1], g, xo), 0.f) & 0xffffu), ro, ovo, 0, 0);
      if (bid == 0 && lane == 0) __hip_atomic_store(reinterpret_cast<unsigned*>(ws + WS_EPOCH), tag, RLX_AGENT);
    }
    asm volatile(".Lfd_code_end:");
  }
}

}

extern "C" void kernel_launch(void* const* d_in, const int* in_sizes, int n_in, void* d_out, int out_size, void* d_ws, size_t ws_size,
                              hipStream_t stream) {
  (void)in_sizes; (void)n_in; (void)out_size; (void)ws_size;
  fused_decode_kernel<<<NWG, NTHR, 0, stream>>>(
      (const unsigned short*)d_in[0], (const unsigned short*)d_in[1], (const unsigned short*)d_in[2], (const unsigned short*)d_in[3],
      (const unsigned short*)d_in[4], (const unsigned short*)d_in[6], (const unsigned short*)d_in[7], (const unsigned short*)d_in[8],
      (const unsigned short*)d_in[9], (unsigned short*)d_out, (unsigned char*)d_ws);
}
```
